```python
import torch

def _softmax(x, axis=-1):
    return torch.softmax(x, dim=axis)
def _gelu(x):
    return torch.nn.functional.gelu(x, approximate="tanh")
def _silu(x):
    return torch.nn.functional.silu(x)
def _sigmoid(x):
    return torch.sigmoid(x)
def _layernorm(x, axis=-1, eps=1e-5):
    m = x.mean(dim=axis, keepdim=True)
    v = x.var(dim=axis, keepdim=True, unbiased=False)
    return (x - m) / torch.sqrt(v + eps)
def _scatter_add(tgt, idx, src, axis=0):
    return tgt.clone().index_add_(axis, idx, src)
def _segment_sum(src, seg, n):
    out = torch.zeros((n,) + src.shape[1:], dtype=src.dtype)
    return out.index_add_(0, seg, src)
def _pad(x, pw):
    flat = tuple(v for pair in reversed(pw) for v in pair)
    return torch.nn.functional.pad(x, flat)

kernel_name = 'recurrent_scan__spectral_fnet__mixer_channel_mlp__prompt'

def setup_inputs():

    _w = (lambda a: (lambda t: t.to(torch.bfloat16) if t.is_floating_point() else t)(torch.as_tensor(a)))
    def _A_setup():
        B, S, D = 1, 2048, 512
        ns = torch.arange(S)[:, None] * torch.arange(S)[None, :]
        nd = torch.arange(D)[:, None] * torch.arange(D)[None, :]
        Ws_re = torch.cos(-2.0 * torch.pi * ns / S).to(float)
        Ws_im = torch.sin(-2.0 * torch.pi * ns / S).to(float)
        Wd_re = torch.cos(-2.0 * torch.pi * nd / D).to(float)
        Wd_im = torch.sin(-2.0 * torch.pi * nd / D).to(float)
        return {
            "x": _w((lambda *s: torch.randn(*s, dtype=torch.bfloat16) * 0.05)(B, S, D)),
            "dft_seq_re": _w(Ws_re),
            "dft_seq_im": _w(Ws_im),
            "dft_hid_re": _w(Wd_re),
            "dft_hid_im": _w(Wd_im),
        }
    _A_in = _A_setup()

    def _B_setup():
        B, S, D, F = 1, 2048, 512, 1376
        return {
            "x": _w((lambda *s: torch.randn(*s, dtype=torch.bfloat16) * 0.05)(B, S, D)),
            "w1": _w((lambda *s: torch.randn(*s, dtype=torch.bfloat16) * 0.8)(D, F) * (D ** -0.5)),
            "b1": _w((lambda *s: torch.randn(*s, dtype=torch.bfloat16) * 0.8)(F)),
            "w2": _w((lambda *s: torch.randn(*s, dtype=torch.bfloat16) * 0.05)(F, D) * (F ** -0.5)),
            "b2": _w((lambda *s: torch.randn(*s, dtype=torch.bfloat16) * 0.05)(D)),
        }
    _B_in = _B_setup()

    return {
        "A_x": _A_in["x"],
        "A_dft_seq_re": _A_in["dft_seq_re"],
        "A_dft_seq_im": _A_in["dft_seq_im"],
        "A_dft_hid_re": _A_in["dft_hid_re"],
        "A_dft_hid_im": _A_in["dft_hid_im"],
        "B_x": _B_in["x"],
        "B_w1": _B_in["w1"],
        "B_b1": _B_in["b1"],
        "B_w2": _B_in["w2"],
        "B_b2": _B_in["b2"],
    }

def reference(A_x, A_dft_seq_re, A_dft_seq_im, A_dft_hid_re, A_dft_hid_im, B_x, B_w1, B_b1, B_w2, B_b2):
    def _A_ref(x, dft_seq_re, dft_seq_im, dft_hid_re, dft_hid_im):
        a = torch.einsum("bsd,ed->bse", x, dft_hid_re)
        b = torch.einsum("bsd,ed->bse", x, dft_hid_im)
        y = torch.einsum("ts,bse->bte", dft_seq_re, a) - torch.einsum("ts,bse->bte", dft_seq_im, b)
        return y

    def _B_ref(x, w1, b1, w2, b2):
        y = _layernorm(x, axis=-1)
        h = _gelu(torch.einsum("bsd,df->bsf", y, w1) + b1)
        return torch.einsum("bsf,fd->bsd", h, w2) + b2

    x = A_x
    xn = _layernorm(x, axis=-1)
    s = _A_ref(xn, A_dft_seq_re, A_dft_seq_im, A_dft_hid_re, A_dft_hid_im)
    g = _silu(_B_ref(xn, B_w1, B_b1, B_w2, B_b2))
    return x + s * g
```

```cpp
#include <hip/hip_runtime.h>
#include <hip/hip_bf16.h>
typedef __hip_bfloat16 bf16;
__device__ __forceinline__ float b2f(bf16 v){ return __bfloat162float(v); }
__device__ __forceinline__ float rbf(float v){ return __bfloat162float(__float2bfloat16(v)); }

__global__ void ln_k(const bf16* x, bf16* xn, bf16* y2){
  int row = blockIdx.x; int lane = threadIdx.x;
  float v[8];
  for(int j=0;j<8;j++) v[j]=b2f(x[row*512+lane*8+j]);
  for(int pass=0;pass<2;pass++){
    float s=0; for(int j=0;j<8;j++) s+=v[j];
    for(int o=32;o>=1;o>>=1) s+=__shfl_xor(s,o);
    float m=s/512.f;
    float q=0; for(int j=0;j<8;j++) q+=(v[j]-m)*(v[j]-m);
    for(int o=32;o>=1;o>>=1) q+=__shfl_xor(q,o);
    float var=rbf(q/512.f); float mb=rbf(m);
    float den=rbf(sqrtf(rbf(var+1e-5f)));
    for(int j=0;j<8;j++) v[j]=rbf(rbf(v[j]-mb)/den);
    bf16* o = pass==0?xn:y2;
    for(int j=0;j<8;j++) o[row*512+lane*8+j]=__float2bfloat16(v[j]);
  }
}
template<int BT,int EPI>
__global__ void gemm_k(const bf16* A,const bf16* B,int M,int N,int K,bf16* C,const bf16* bias){
  int n=blockIdx.x*64+threadIdx.x%64; int m=blockIdx.y*4+threadIdx.x/64;
  if(n>=N) return;
  float acc=0;
  for(int k=0;k<K;k++) acc+=b2f(A[(long)m*K+k])*b2f(BT?B[(long)n*K+k]:B[(long)k*N+n]);
  acc=rbf(acc);
  if(EPI>=1) acc=rbf(acc+b2f(bias[n]));
  if(EPI==1){ float u=acc; acc=0.5f*u*(1.f+tanhf(0.7978845608028654f*(u+0.044715f*u*u*u))); }
  if(EPI==2){ acc=acc/(1.f+expf(-acc)); }
  C[(long)m*N+n]=__float2bfloat16(acc);
}
__global__ void fin_k(const bf16* x,const bf16* p,const bf16* q,const bf16* g,bf16* out,int n){
  int i=blockIdx.x*256+threadIdx.x; if(i>=n) return;
  float s=rbf(b2f(p[i])-b2f(q[i]));
  out[i]=__float2bfloat16(b2f(x[i])+rbf(s*b2f(g[i])));
}
extern "C" void kernel_launch(void* const* d_in, const int* in_sizes, int n_in,
                              void* d_out, int out_size, void* d_ws, size_t ws_size,
                              hipStream_t stream) {
  const bf16* x=(const bf16*)d_in[0]; const bf16* Cr=(const bf16*)d_in[1]; const bf16* Ci=(const bf16*)d_in[2];
  const bf16* Wr=(const bf16*)d_in[3]; const bf16* Wi=(const bf16*)d_in[4];
  const bf16* w1=(const bf16*)d_in[6]; const bf16* b1=(const bf16*)d_in[7]; const bf16* w2=(const bf16*)d_in[8]; const bf16* b2=(const bf16*)d_in[9];
  bf16* ws=(bf16*)d_ws; const long T=2048*512;
  bf16 *xn=ws,*y2=ws+T,*a=ws+2*T,*b=ws+3*T,*p=ws+4*T,*q=ws+5*T,*g=ws+6*T,*h=ws+7*T;
  ln_k<<<2048,64,0,stream>>>(x,xn,y2);
  gemm_k<1,0><<<dim3(8,512),256,0,stream>>>(xn,Wr,2048,512,512,a,nullptr);
  gemm_k<1,0><<<dim3(8,512),256,0,stream>>>(xn,Wi,2048,512,512,b,nullptr);
  gemm_k<0,0><<<dim3(8,512),256,0,stream>>>(Cr,a,2048,512,2048,p,nullptr);
  gemm_k<0,0><<<dim3(8,512),256,0,stream>>>(Ci,b,2048,512,2048,q,nullptr);
  gemm_k<0,1><<<dim3(22,512),256,0,stream>>>(y2,w1,2048,1376,512,h,b1);
  gemm_k<0,2><<<dim3(8,512),256,0,stream>>>(h,w2,2048,512,1376,g,b2);
  fin_k<<<T/256,256,0,stream>>>(x,p,q,g,(bf16*)d_out,(int)T);
}
```

```cpp
#include <hip/hip_runtime.h>
#include <hip/hip_bf16.h>
#include <stdint.h>

typedef unsigned short u16;
typedef unsigned int u32;
typedef unsigned long long u64;
typedef __attribute__((ext_vector_type(8))) short bf16x8;
typedef __attribute__((ext_vector_type(4))) short s16x4;
typedef __attribute__((ext_vector_type(2))) __bf16 bf2;
typedef __attribute__((ext_vector_type(4))) float f32x4;
typedef __attribute__((ext_vector_type(2))) float f32x2;
typedef __attribute__((ext_vector_type(16))) float f32x16;
typedef __attribute__((ext_vector_type(4))) unsigned u32x4;
typedef __attribute__((ext_vector_type(2))) unsigned u32x2;
#define LAS __attribute__((address_space(3)))
#define GAS __attribute__((address_space(1)))
typedef GAS u64 gu64;

#define PBF16 1
constexpr u32 WS_EPOCH = 0;
constexpr u32 WS_FAB = 4096;
constexpr u32 WS_FH = WS_FAB + 16384;
constexpr u32 WS_FP = WS_FH + 16384;
constexpr u32 WS_AB = 1u << 20;
constexpr u32 WS_H = WS_AB + (4u << 20);
constexpr u32 WS_P = WS_H + (6u << 20);
constexpr u32 WS_DBG = WS_P + (16u << 20);
constexpr u32 WS_END = WS_DBG + (2u << 20);

constexpr u32 BLK = 16384;
constexpr u32 MISC = 9 * BLK;
constexpr u32 M_STAT = MISC;
constexpr u32 M_WS = MISC + 1024;
constexpr u32 M_CS = MISC + 2048;
constexpr u32 M_DUMMY = MISC + 4096;

#define RLX_AGENT __ATOMIC_RELAXED, __HIP_MEMORY_SCOPE_AGENT
#define WAIT_V(n) asm volatile("s_waitcnt vmcnt(" #n ")" ::: "memory")
#define WAIT_L0() asm volatile("s_waitcnt lgkmcnt(0)" ::: "memory")
#define BAR() __builtin_amdgcn_s_barrier()
#define SB0() __builtin_amdgcn_sched_barrier(0)
#define RFL(x) ((u32)__builtin_amdgcn_readfirstlane((u32)(x)))
#define BARS(i) BAR()
constexpr u32 SPIN_CAP = 1u << 20;

#define STAMP(i) do {} while (0)
#define STAMPD(i) do {} while (0)
#define STAMP_DECL
#define STAMP_FLUSH() do {} while (0)

__device__ __forceinline__ float rbf(float v) { return (float)(__bf16)v; }
__device__ __forceinline__ u32 pack2(float a, float b) { bf2 v = {(__bf16)a, (__bf16)b}; return __builtin_bit_cast(u32, v); }
__device__ __forceinline__ float lo_f(u32 v) { return __builtin_bit_cast(float, v << 16); }
__device__ __forceinline__ float hi_f(u32 v) { return __builtin_bit_cast(float, v & 0xffff0000u); }
__device__ __forceinline__ float sigm(float z) {
  return __builtin_amdgcn_rcpf(1.0f + __builtin_amdgcn_exp2f(-1.4426950408889634f * z));
}
__device__ __forceinline__ float gelu_t(float u) {
  const float z = u * (-2.3022081983f - 0.1029432398f * u * u);
  return u * __builtin_amdgcn_rcpf(1.0f + __builtin_amdgcn_exp2f(z));
}
__device__ __forceinline__ u32x4 mk_rs(const void* p, u32 bytes) {
  const u64 a = (u64)p;
  u32x4 r = {RFL(a), RFL((u32)(a >> 32) & 0xffffu), bytes, 0x00020000u};
  return r;
}
#define DMA4M(MOD, rs, d, v0, v1, v2, v3, so) do { u32 keep_; \
  asm volatile("s_nop 4\n\ts_mov_b32 %0, m0\n\ts_mov_b32 m0, %1\n\ts_nop 0\n\t" \
               "buffer_load_dwordx4 %2, %6, %7 offen " MOD " lds\n\t" \
               "buffer_load_dwordx4 %3, %6, %7 offen offset:1024 " MOD " lds\n\t" \
               "buffer_load_dwordx4 %4, %6, %7 offen offset:2048 " MOD " lds\n\t" \
               "buffer_load_dwordx4 %5, %6, %7 offen offset:3072 " MOD " lds\n\t" \
               "s_mov_b32 m0, %0" \
               : "=&s"(keep_) : "s"(RFL(d)), "v"(v0), "v"(v1), "v"(v2), "v"(v3), "s"(rs), "s"(RFL(so)) : "memory"); } while (0)
#define DMA4(rs, d, v0, v1, v2, v3, so) DMA4M("", rs, d, v0, v1, v2, v3, so)
#define DMA4S(rs, d, v0, v1, v2, v3, so) DMA4M("sc1", rs, d, v0, v1, v2, v3, so)
#define DMA2(rs, d, v0, v1, so) do { u32 keep_; \
  asm volatile("s_nop 4\n\ts_mov_b32 %0, m0\n\ts_mov_b32 m0, %1\n\ts_nop 0\n\t" \
               "buffer_load_dwordx4 %2, %4, %5 offen lds\n\t" \
               "buffer_load_dwordx4 %3, %4, %5 offen offset:1024 lds\n\t" \
               "s_mov_b32 m0, %0" \
               : "=&s"(keep_) : "s"(RFL(d)), "v"(v0), "v"(v1), "s"(rs), "s"(RFL(so)) : "memory"); } while (0)
#define TOUCH(rs, d, v0, so) do { u32 keep_; \
  asm volatile("s_nop 4\n\ts_mov_b32 %0, m0\n\ts_mov_b32 m0, %1\n\ts_nop 0\n\t" \
               "buffer_load_dword %2, %3, %4 offen lds\n\t" \
               "s_mov_b32 m0, %0" \
               : "=&s"(keep_) : "s"(RFL(d)), "v"(v0), "s"(rs), "s"(RFL(so)) : "memory"); } while (0)
#define TOUCHG(d, p) do { u32 keep_; \
  asm volatile("s_mov_b32 %0, m0\n\ts_mov_b32 m0, %1\n\ts_nop 0\n\t" \
               "global_load_lds_dword %2, off\n\t" \
               "s_mov_b32 m0, %0" \
               : "=&s"(keep_) : "s"(RFL(d)), "v"(p) : "memory"); } while (0)
#define MFMA(a, b, c) __builtin_amdgcn_mfma_f32_16x16x32_bf16(a, b, c, 0, 0, 0)
#define MFMA32(a, b, c) __builtin_amdgcn_mfma_f32_32x32x16_bf16(a, b, c, 0, 0, 0)

template <int NPER>
__device__ __forceinline__ void poll_flags(gu64* f, u64 tag, bool skip = false) {
  for (u32 sp = 0; sp < SPIN_CAP; ++sp) {
    bool ok = true;
#pragma unroll
    for (int k = 0; k < NPER; ++k) {
      u64 v = __hip_atomic_load(f + k * 64, RLX_AGENT);
      ok &= (v == tag);
    }
    if (__all(ok || skip)) break;
    __builtin_amdgcn_s_sleep(1);
  }
  asm volatile("" ::: "memory");
}
#define POLL_ISSUE(val, ptr) asm volatile("global_load_dwordx2 %0, %1, off sc1" : "=v"(val) : "v"(ptr) : "memory")

__global__ __launch_bounds__(512, 2) void fused_k(const u16* __restrict__ x, const u16* __restrict__ Cr, const u16* __restrict__ Ci,
                                                   const u16* __restrict__ Wr, const u16* __restrict__ Wi, const u16* __restrict__ w1,
                                                   const u16* __restrict__ b1, const u16* __restrict__ w2, const u16* __restrict__ b2,
                                                   u16* __restrict__ out, char* __restrict__ ws) {
  __shared__ __attribute__((aligned(1024))) char smem[163840];
  const int tid = threadIdx.x, lane = tid & 63;
  const int wave = __builtin_amdgcn_readfirstlane(tid >> 6);
  const int bid = blockIdx.x, xcd = bid & 7, idx = bid >> 3;
  const int stile = (xcd & 1) * 8 + (idx & 7);
  const int sg = xcd >> 1;
  const int nt2 = idx >> 3;
  const int cg = sg * 4 + nt2;
  const int g = wave >> 2, r = wave & 3;
  const int i16 = lane & 15, q = lane >> 4;
  LAS char* L = (LAS char*)smem;
  const u32 lb = (u32)(size_t)L;
  STAMP_DECL;
  STAMP(0);

  const __amdgpu_buffer_rsrc_t ws_rs = __builtin_amdgcn_make_buffer_rsrc((void*)ws, 0, WS_END, 0x00020000);
  const u32x4 ws_r4 = mk_rs(ws, WS_END);
  const u32x4 c_rs = mk_rs((sg < 2 ? Cr : Ci) + (size_t)stile * 128 * 2048 + (sg & 1) * 1024, 128 * 4096);

  const int nh = cg < 11 ? 3 : 2;
  const int hu0 = cg < 11 ? 3 * cg : 33 + 2 * (cg - 11);
  const int hu2 = cg < 11 ? hu0 + 2 : hu0;
  u32x2 bvA[2], bvC[2];
#pragma unroll
  for (int ft = 0; ft < 2; ++ft) {
    asm volatile("global_load_dwordx2 %0, %1, off" : "=v"(bvA[ft]) : "v"(b1 + (hu0 + g) * 32 + ft * 16 + 4 * q) : "memory");
    asm volatile("global_load_dwordx2 %0, %1, off" : "=v"(bvC[ft]) : "v"(b1 + hu2 * 32 + ft * 16 + 4 * q) : "memory");
  }
  u64 tag;
  POLL_ISSUE(tag, (gu64*)(ws + WS_EPOCH));

  const u32x4 x_rs = mk_rs(x + (size_t)stile * 128 * 512, 131072);
  const u32x4 w_rs = mk_rs((cg < 8 ? Wr : Wi) + (size_t)(cg & 7) * 64 * 512, 65536);
  int xq[4], wq[2];
#pragma unroll
  for (int ii = 0; ii < 4; ++ii) xq[ii] = (wave * 16 + 4 * ii + q) * 1024 + ((i16 ^ (4 * ii + q)) << 4) - ii * 1024;
#pragma unroll
  for (int ii = 0; ii < 2; ++ii) wq[ii] = (wave * 8 + 4 * ii + q) * 1024 + ((i16 ^ ((wave * 8 + 4 * ii + q) & 15)) << 4) - ii * 1024;
  {
#pragma unroll
    for (int kq = 0; kq < 3; ++kq) {
      DMA4(x_rs, lb + kq * 3 * BLK + wave * 4096, xq[0], xq[1], xq[2], xq[3], kq * 256);
      DMA2(w_rs, lb + kq * 3 * BLK + 2 * BLK + wave * 2048, wq[0], wq[1], kq * 256);
      if (kq < 2) BAR();
    }
  }
  STAMP(10);

  const u32x4 w1_rs = mk_rs(w1, 512 * 1376 * 2);
  constexpr int W1ST = 16 * 2752 - 1024;
  const int w1vo = (lane >> 2) * 2752 + (((((lane & 3) >> 1) ^ (lane >> 5)) & 1) << 5) + ((lane & 1) << 4);
  auto issue_h = [&](int unit, u32 d0, u32 d1) {
    DMA4(w1_rs, lb + (g ? d1 : d0) + r * 4096, w1vo, w1vo + W1ST, w1vo + 2 * W1ST, w1vo + 3 * W1ST, wave * 64 * 2752 + unit * 64);
  };

  const bf16x8 ones = {0x3f80, 0x3f80, 0x3f80, 0x3f80, 0x3f80, 0x3f80, 0x3f80, 0x3f80};
  bf16x8 xf[2][16];
  f32x4 accA[2][2] = {};
  f32x4 sS = {}, sQ = {}, wsA[2] = {};
  {
    u32 sw[4];
#pragma unroll
    for (int k3 = 0; k3 < 4; ++k3) sw[k3] = i16 * 256 + (((k3 * 4 + q) ^ i16) << 4);
    const u32 xo0 = r * 8192 + g * 4096, xo1 = r * 8192 + (g ^ 1) * 4096, wo = 2 * BLK + g * 8192;
    bf16x8 wf[2][4][2];
#pragma unroll
    for (int kq = 0; kq < 5; ++kq) {
      if (kq < 4) {
        if (kq == 0) WAIT_V(12); else if (kq == 1) WAIT_V(6); else if (kq == 2) WAIT_V(6); else WAIT_V(4);
        if (kq == 0) { asm volatile("" : "+v"(bvA[0]), "+v"(bvA[1]), "+v"(bvC[0]), "+v"(bvC[1]), "+v"(tag)); tag += 1ull; }
        WAIT_L0();
        BAR();
        if (kq == 0) STAMP(1);
        if (kq == 1) STAMP(11);
        if (kq == 2) STAMP(12);
        if (kq == 3) STAMP(13);
      }
      {
        const u32 qb = (kq % 3) * 3 * BLK;
        const int kp = kq - 1;
#pragma unroll
        for (int k3 = 0; k3 < 4; ++k3) {
          if (kq < 4) {
            xf[0][kq * 4 + k3] = *(const LAS bf16x8*)(L + qb + xo0 + sw[k3]);
            xf[1][kq * 4 + k3] = *(const LAS bf16x8*)(L + qb + xo1 + sw[k3]);
#pragma unroll
            for (int nt = 0; nt < 2; ++nt) wf[kq & 1][k3][nt] = *(const LAS bf16x8*)(L + qb + wo + nt * 4096 + sw[k3]);
          }
          if (kq > 0) {
            const int ks = kp * 4 + k3;
#pragma unroll
            for (int nt = 0; nt < 2; ++nt) {
              accA[nt][0] = MFMA(xf[0][ks], wf[kp & 1][k3][nt], accA[nt][0]);
              accA[nt][1] = MFMA(xf[1][ks], wf[kp & 1][k3][nt], accA[nt][1]);
            }
            sS = MFMA(ones, xf[0][ks], sS);
            sQ = MFMA(xf[0][ks], xf[0][ks], sQ);
            if (kq < 4) {
#pragma unroll
              for (int i = 0; i < 4; ++i) {
                __builtin_amdgcn_sched_group_barrier(0x008, 1, 0);
                __builtin_amdgcn_sched_group_barrier(0x100, 1, 0);
              }
              __builtin_amdgcn_sched_group_barrier(0x008, 2, 0);
            }
          }
          SB0();
          if (kq == 1 && k3 == 0) DMA4(x_rs, lb + wave * 4096, xq[0], xq[1], xq[2], xq[3], 768);
          if (kq == 1 && k3 == 1) DMA2(w_rs, lb + 2 * BLK + wave * 2048, wq[0], wq[1], 768);
          if (kq == 2 && k3 == 0) issue_h(hu0, 3 * BLK, 4 * BLK);
          if (kq == 3 && k3 == 0) issue_h(hu0 + 1, 6 * BLK, 7 * BLK);
          if (kq == 3 && k3 == 1) issue_h(hu2, 5 * BLK, 8 * BLK);
          SB0();
        }
      }
      if (kq > 0) {
        const int kp = kq - 1;
        if (r == kp) {
#pragma unroll
          for (int k3 = 0; k3 < 4; ++k3)
#pragma unroll
            for (int nt = 0; nt < 2; ++nt) wsA[nt] = MFMA(ones, wf[kp & 1][k3][nt], wsA[nt]);
        }
        SB0();
      }
    }
  }
  {
    const int j = i16 & 3;
    float d = j == 0 ? sQ[0] : j == 1 ? sQ[1] : j == 2 ? sQ[2] : sQ[3];
    if (q == (i16 >> 2)) { f32x2 v = {sS[0], d}; *(LAS f32x2*)(L + M_STAT + (r * 32 + g * 16 + i16) * 8) = v; }
    if (q == 0) {
      *(LAS float*)(L + M_WS + ((g * 4 + r) * 32 + i16) * 4) = wsA[0][0];
      *(LAS float*)(L + M_WS + ((g * 4 + r) * 32 + 16 + i16) * 4) = wsA[1][0];
    }
  }
  WAIT_V(4);
  WAIT_L0();
  BAR();
  STAMP(2);

  int cvo[4];
#pragma unroll
  for (int ii = 0; ii < 4; ++ii)
    cvo[ii] = ((r * 4 + ii) * 8 + (lane >> 3)) * 4096 + (((lane & 7) ^ (((ii & 1) * 4 + (lane >> 4)) & 7)) << 4) - ii * 1024;
  const int lv = lane * 16;
  auto blkC = [&](int st) -> u32 { return (u32)(st & 3) * BLK; };
  auto blkAB = [&](int st) -> u32 { return ((0x5764u >> ((st & 3) * 4)) & 15u) * BLK; };
  const int rot = 2 * (stile & 7) + (nt2 & 1) + 8 * (nt2 >> 1);
  auto issue_c = [&](int st) { DMA4(c_rs, lb + blkC(st) + r * 4096, cvo[0], cvo[1], cvo[2], cvo[3], ((st + rot) & 15) * 128); };
  const u32 ab_src = WS_AB + (((sg >> 1) * 4 + nt2) * 64 + (sg & 1) * 32) * 8192 + r * 4096;
  auto issue_ab = [&](int st) { DMA4S(ws_r4, lb + blkAB(st) + r * 4096, lv, lv, lv, lv, ab_src + ((st + rot) & 15) * 16384); };

  const int c4 = (q & 1) * 2 + (q >> 1);
  float mH[2], rH[2];
  float mA[2], rA[2], mrow[2][4], rrow[2][4], wsv[2];
  auto epiA = [&](int ks) {
    if (ks == 0) {
#pragma unroll
      for (int mt = 0; mt < 2; ++mt) {
        const f32x2 sv = *(const LAS f32x2*)(L + M_STAT + (r * 32 + (mt ^ g) * 16 + i16) * 8);
        const float mean = sv[0] * (1.f / 512.f);
        const float var = fmaxf(sv[1] * (1.f / 512.f) - mean * mean, 0.f);
        const float mb = rbf(mean);
        const float den = rbf(__builtin_amdgcn_sqrtf(rbf(rbf(var) + 1e-5f)));
        const float inv = __builtin_amdgcn_rcpf(den);
        const float m2 = rbf((mean - mb) * inv);
        const float den2 = rbf(__builtin_amdgcn_sqrtf(rbf(rbf(var * inv * inv) + 1e-5f)));
        mA[mt] = mb; rA[mt] = cg < 8 ? inv : -inv;
        rH[mt] = inv * __builtin_amdgcn_rcpf(den2); mH[mt] = (mb + m2 * den) * rH[mt];
      }
    } else if (ks == 1) {
#pragma unroll
      for (int mt = 0; mt < 2; ++mt)
#pragma unroll
        for (int j = 0; j < 4; ++j) { mrow[mt][j] = __shfl(mA[mt], 4 * q + j); rrow[mt][j] = __shfl(rA[mt], 4 * q + j); }
#pragma unroll
      for (int nt = 0; nt < 2; ++nt) {
        wsv[nt] = 0.f;
#pragma unroll
        for (int rr = 0; rr < 4; ++rr) wsv[nt] += *(const LAS float*)(L + M_WS + ((g * 4 + rr) * 32 + nt * 16 + i16) * 4);
      }
    } else if (ks == 3 || ks == 5) {
      const int nt = ks == 5;
      const u32 dst = WS_AB + ((((cg >> 3) * 4 + ((cg & 7) >> 1)) * 64 + stile * 4 + r) * 8 + (cg & 1) * 4 + g * 2) * 1024 + ((c4 ^ (g * 2)) * 16 + i16) * 16;
      u32 p[2][2];
#pragma unroll
      for (int mt = 0; mt < 2; ++mt) {
        float v[4];
#pragma unroll
        for (int j = 0; j < 4; ++j) v[j] = (accA[nt][mt][j] - mrow[mt][j] * wsv[nt]) * rrow[mt][j];
        p[mt][0] = pack2(v[0], v[1]); p[mt][1] = pack2(v[2], v[3]);
      }
      auto s0 = __builtin_amdgcn_permlane16_swap(p[0][0], p[1][0], false, false);
      auto s1 = __builtin_amdgcn_permlane16_swap(p[0][1], p[1][1], false, false);
      u32x4 sv = {s0[0], s1[0], s0[1], s1[1]};
      __builtin_amdgcn_raw_buffer_store_b128(sv, ws_rs, dst + nt * 1024, 0, 16);
    }
  };
  epiA(0); epiA(1); epiA(3); epiA(5);
  SB0();
  auto sideB = [&](int ks) {
    if (ks == 6) {
      WAIT_V(0);
      if (lane == 0)
        __hip_atomic_store((gu64*)(ws + WS_FAB) + (((((cg >> 3) * 4 + ((cg & 7) >> 1)) * 16 + stile) * 2 + (cg & 1)) * 8 + wave), tag, RLX_AGENT);
    } else if (ks == 8 || ks == 10 || ks == 12) {
      if (g == 0) issue_c((ks - 8) >> 1);
    }
  };

  auto h_mma = [&](f32x4 (&acc)[2][2], u32 d0, u32 d1, int slot, auto&& side) {
    const int a = i16 >> 2, p = lane & 3;
    u32 tb[2][2];
#pragma unroll
    for (int ft = 0; ft < 2; ++ft) {
      const u32 o = (8 * q + a) * 64 + ((ft ^ (q & 1)) * 32) + p * 8;
      tb[0][ft] = d0 + o; tb[1][ft] = d1 + o;
      acc[ft][0] = (f32x4){0.f, 0.f, 0.f, 0.f}; acc[ft][1] = (f32x4){0.f, 0.f, 0.f, 0.f};
    }
    {
      f32x4 cs[2] = {};
      const u32 cb = (r < 2 ? d0 : d1) + (r & 1) * 8192 - d0;
#pragma unroll
      for (int k = 0; k < 4; ++k)
#pragma unroll
        for (int ft = 0; ft < 2; ++ft) {
          s16x4 lo = __builtin_amdgcn_ds_read_tr16_b64_v4i16((LAS s16x4*)(L + tb[0][ft] + cb + k * 2048));
          s16x4 hi = __builtin_amdgcn_ds_read_tr16_b64_v4i16((LAS s16x4*)(L + tb[0][ft] + cb + k * 2048 + 256));
          cs[ft] = MFMA(__builtin_shufflevector(lo, hi, 0, 1, 2, 3, 4, 5, 6, 7), ones, cs[ft]);
        }
      if (i16 == 0) {
        *(LAS f32x4*)(L + M_CS + ((slot * 4 + r) * 32 + 4 * q) * 4) = cs[0];
        *(LAS f32x4*)(L + M_CS + ((slot * 4 + r) * 32 + 16 + 4 * q) * 4) = cs[1];
      }
    }
    bf16x8 wf[3][2];
    auto ldw = [&](int ks) {
#pragma unroll
      for (int ft = 0; ft < 2; ++ft) {
        s16x4 lo = __builtin_amdgcn_ds_read_tr16_b64_v4i16((LAS s16x4*)(L + tb[ks >> 3][ft] + (ks & 7) * 2048));
        s16x4 hi = __builtin_amdgcn_ds_read_tr16_b64_v4i16((LAS s16x4*)(L + tb[ks >> 3][ft] + (ks & 7) * 2048 + 256));
        wf[ks % 3][ft] = __builtin_shufflevector(lo, hi, 0, 1, 2, 3, 4, 5, 6, 7);
      }
    };
    ldw(0); ldw(1);
    SB0();
#pragma unroll
    for (int ks = 0; ks < 16; ++ks) {
      if (ks + 2 < 16) ldw(ks + 2);
#pragma unroll
      for (int ft = 0; ft < 2; ++ft) {
        acc[ft][0] = MFMA(wf[ks % 3][ft], xf[0][ks], acc[ft][0]);
        acc[ft][1] = MFMA(wf[ks % 3][ft], xf[1][ks], acc[ft][1]);
      }
      side(ks);
      SB0();
    }
  };
  auto preH = [&](f32x4 (&acc)[2][2], int slot, const u32x2* bv, u32 (&pa)[2][2][2]) {
#pragma unroll
    for (int ft = 0; ft < 2; ++ft) {
      f32x4 ecs = *(const LAS f32x4*)(L + M_CS + ((slot * 4 + 0) * 32 + ft * 16 + 4 * q) * 4);
#pragma unroll
      for (int rr = 1; rr < 4; ++rr) ecs += *(const LAS f32x4*)(L + M_CS + ((slot * 4 + rr) * 32 + ft * 16 + 4 * q) * 4);
      const float bj[4] = {lo_f(bv[ft][0]), hi_f(bv[ft][0]), lo_f(bv[ft][1]), hi_f(bv[ft][1])};
#pragma unroll
      for (int mt = 0; mt < 2; ++mt) {
        float v[4];
#pragma unroll
        for (int jj = 0; jj < 4; ++jj) v[jj] = acc[ft][mt][jj] * rH[mt] + (bj[jj] - mH[mt] * ecs[jj]);
        pa[mt][ft][0] = pack2(v[0], v[1]); pa[mt][ft][1] = pack2(v[2], v[3]);
      }
    }
  };
  auto actH = [&](u32 p00, u32 p01, u32 p10, u32 p11, int unit, int trow16) {
    const u32 dst = WS_H + (((stile * 2 + (r >> 1)) * 43 + unit) * 4 + (r & 1) * 2 + trow16) * 1024 + (c4 * 16 + i16) * 16;
    const u32 k00 = pack2(gelu_t(lo_f(p00)), gelu_t(hi_f(p00))), k01 = pack2(gelu_t(lo_f(p01)), gelu_t(hi_f(p01)));
    const u32 k10 = pack2(gelu_t(lo_f(p10)), gelu_t(hi_f(p10))), k11 = pack2(gelu_t(lo_f(p11)), gelu_t(hi_f(p11)));
    auto s0 = __builtin_amdgcn_permlane16_swap(k00, k10, false, false);
    auto s1 = __builtin_amdgcn_permlane16_swap(k01, k11, false, false);
    u32x4 sv = {s0[0], s1[0], s0[1], s1[1]};
    __builtin_amdgcn_raw_buffer_store_b128(sv, ws_rs, dst, 0, 16);
  };
  auto act2 = [&](u32 p) -> u32 { return pack2(gelu_t(lo_f(p)), gelu_t(hi_f(p))); };
  auto actH_fin = [&](u32 k00, u32 k01, u32 p10, u32 p11, int unit, int trow16) {
    const u32 dst = WS_H + (((stile * 2 + (r >> 1)) * 43 + unit) * 4 + (r & 1) * 2 + trow16) * 1024 + (c4 * 16 + i16) * 16;
    auto s0 = __builtin_amdgcn_permlane16_swap(k00, act2(p10), false, false);
    auto s1 = __builtin_amdgcn_permlane16_swap(k01, act2(p11), false, false);
    u32x4 sv = {s0[0], s1[0], s0[1], s1[1]};
    __builtin_amdgcn_raw_buffer_store_b128(sv, ws_rs, dst, 0, 16);
  };
  f32x4 accB[2][2], accC[2][2];
  h_mma(accB, (3 + 3 * g) * BLK, (4 + 3 * g) * BLK, g, sideB);
  STAMP(14);
  WAIT_L0();
  BAR();
  STAMP(3);

  const int tile = stile * 4 + nt2;
  if (g == 1) {
    u32 pa[2][2][2];
    preH(accB, 1, bvA, pa);
    poll_flags<2>((gu64*)(ws + WS_FAB) + (((sg >> 1) * 4 + nt2) * 16 + (sg & 1) * 8) * 16 + lane, tag);
    STAMP(4);
    issue_ab(0); issue_ab(1); issue_ab(2); issue_c(3);
    const u32 h_src = WS_H + (stile * 2 + (sg >> 1)) * 43 * 4096;
    const u32x4 w2_rs = mk_rs(w2, 1376 * 512 * 2);
    int wvo[4];
#pragma unroll
    for (int ii = 0; ii < 4; ++ii) {
      const int c = lane & 7, f = ((lane >> 4) & 1) << 1;
      wvo[ii] = (ii * 8 + (lane >> 3)) * 1024 + (nt2 * 128 + (sg & 1) * 64) * 2 + ((((c >> 1) ^ f) << 5) | ((c & 1) << 4)) - ii * 1024;
    }
    const int orot = stile + nt2;
    auto issue_o = [&](int j) {
      int unit;
      if (j < 4) {
        const int c = ((j + orot) & 3) * 4 + r;
        unit = c < 11 ? 3 * c + 1 : 2 * c + 12;
      } else {
        int jj = j - 4 + (orot & 3) + ((orot >> 2) & 1); jj = jj >= 6 ? jj - 6 : jj; jj = j == 10 ? 6 : jj;
        const int t = min(jj * 4 + r, 26);
        unit = t < 22 ? 3 * (t >> 1) + 2 * (t & 1) : 2 * t - 11;
      }
      DMA4S(ws_r4, lb + blkC(j) + r * 4096, lv, lv, lv, lv, h_src + unit * 4096);
      DMA4(w2_rs, lb + blkAB(j) + r * 4096, wvo[0], wvo[1], wvo[2], wvo[3], unit * 32768);
    };
    BAR();
    issue_ab(3);
    actH(pa[0][0][0], pa[0][0][1], pa[0][1][0], pa[0][1][1], hu0 + 1, 1);
    actH(pa[1][0][0], pa[1][0][1], pa[1][1][0], pa[1][1][1], hu0 + 1, 0);
    WAIT_V(18); BARS(0);
    STAMP(5);
    u32x4 pk_;
    u32 ka, kb;
    auto half_a = [&](int u, int mt) {
      pk_ = *(const LAS u32x4*)(L + 8 * BLK + ((u * 4 + r) * 2 + mt) * 1024 + lane * 16);
      ka = act2(pk_[0]); kb = act2(pk_[1]);
      asm volatile("" : "+v"(ka), "+v"(kb));
    };
    auto half_b = [&](int mt, int unit) { actH_fin(ka, kb, pk_[2], pk_[3], unit, mt); };
    WAIT_V(14); BARS(1); issue_c(4); issue_ab(4);
    half_a(0, 0);
    WAIT_V(8); BARS(2);
    if (lane == 0) __hip_atomic_store((gu64*)(ws + WS_FH) + ((stile * 4 + r) * 16 + cg), tag, RLX_AGENT);
    issue_c(5); issue_ab(5);
    half_b(0, hu0);
    WAIT_V(16); BARS(3); issue_c(6); issue_ab(6);
    half_a(0, 1);
    WAIT_V(16); BARS(4); issue_c(7); issue_ab(7);
    half_b(1, hu0);
    WAIT_V(16); BARS(5); issue_c(8); issue_ab(8);
    if (nh == 3) half_a(1, 0);
    WAIT_V(16); BARS(6); issue_c(9); issue_ab(9);
    if (nh == 3) half_b(0, hu2);
    WAIT_V(16); BARS(7); issue_c(10); issue_ab(10);
    if (nh == 3) half_a(1, 1);
    WAIT_V(16); BARS(8); issue_c(11); issue_ab(11);
    if (nh == 3) half_b(1, hu2);
    WAIT_V(16); BARS(9); issue_c(12); issue_ab(12);
    {
      const int row = (stile & 7) * 172 + min(r * 64 + lane, 171);
      TOUCH(w2_rs, lb + 8 * BLK + r * 256, row * 1024 + (nt2 * 128 + (sg & 1) * 64) * 2, 0);
    }
    gu64* fhp = (gu64*)(ws + WS_FH) + (stile * 4 + 2 * (sg >> 1)) * 16 + (lane & 31);
    u64 pv;
    WAIT_V(8); BARS(10);
    if (lane == 0) __hip_atomic_store((gu64*)(ws + WS_FH) + 1024 + ((stile * 4 + r) * 16 + cg), tag, RLX_AGENT);
    POLL_ISSUE(pv, fhp);
    issue_c(13); issue_ab(13);
    WAIT_V(16); BARS(11); issue_c(14); issue_ab(14);
    WAIT_V(16); BARS(12); issue_c(15); issue_ab(15);
    asm volatile("" : "+v"(pv));
    if (!__all(pv == tag)) poll_flags<1>(fhp, tag);
    WAIT_V(16); BARS(13); issue_o(0);
    WAIT_V(16); BARS(14); POLL_ISSUE(pv, fhp + 1024); issue_o(1);
    WAIT_V(16); BARS(15); issue_o(2);
    WAIT_V(16); BARS(16); issue_o(3);
    asm volatile("" : "+v"(pv));
    if (!__all(pv == tag)) poll_flags<1>(fhp + 1024, tag);
#pragma unroll 1
    for (int i = 17; i < 21; ++i) { WAIT_V(16); BARS(i); issue_o(i - 13); }
    gu64* fpp = (gu64*)(ws + WS_FP) + (tile * 4 + sg) * 8 + (lane & 3);
    WAIT_V(16); BARS(21); issue_o(8);
    WAIT_V(16); BARS(22); POLL_ISSUE(pv, fpp); issue_o(9);
    WAIT_V(16); BARS(23); issue_o(10);
    const u32 p_src = WS_P + (tile * 4 + sg) * 65536 + r * 4096;
    WAIT_V(16); BARS(24);
    asm volatile("" : "+v"(pv));
    if (!__all(pv == tag || (lane & 3) == sg)) poll_flags<1>(fpp, tag, (lane & 3) == sg);
    DMA4S(ws_r4, lb + blkC(3) + r * 4096, lv, lv, lv, lv, WS_P + (tile * 4 + sg) * 65536 + (sg ^ (2 + (r >> 1))) * 16384 + (r & 1) * 4096);
    if (r < 2) {
      DMA4S(ws_r4, lb + 8 * BLK + r * 4096, lv, lv, lv, lv, WS_P + (tile * 4 + sg) * 65536 + (sg ^ 1) * 16384 + r * 4096);
    } else {
      int xo[4];
#pragma unroll
      for (int ii = 0; ii < 4; ++ii) xo[ii] = (ii * 8 + (lane >> 3)) * 1024 + (((lane & 7) ^ ((ii * 4 + (lane >> 4)) & 7)) << 4) - ii * 1024;
      DMA4(x_rs, lb + 8 * BLK + r * 4096, xo[0], xo[1], xo[2], xo[3], ((sg >> 1) * 64 + (r - 2) * 32) * 1024 + (nt2 * 128 + (sg & 1) * 64) * 2);
    }
    WAIT_V(16); BARS(25);
    WAIT_V(8); BARS(26);
    WAIT_V(0); BARS(27);
    BAR();
    STAMP(6);
  } else {
    u32 paB[2][2][2];
    preH(accB, 0, bvA, paB);
    if (nh == 3) h_mma(accC, 5 * BLK, 8 * BLK, 2, [&](int) {});
    WAIT_L0();
    BAR();
#pragma unroll
    for (int mt = 0; mt < 2; ++mt) {
      u32x4 v = {paB[mt][0][0], paB[mt][0][1], paB[mt][1][0], paB[mt][1][1]};
      *(LAS u32x4*)(L + 8 * BLK + (r * 2 + mt) * 1024 + lane * 16) = v;
    }
    if (nh == 3) {
      u32 paC[2][2][2];
      preH(accC, 2, bvC, paC);
#pragma unroll
      for (int mt = 0; mt < 2; ++mt) {
        u32x4 v = {paC[mt][0][0], paC[mt][0][1], paC[mt][1][0], paC[mt][1][1]};
        *(LAS u32x4*)(L + 8 * BLK + ((4 + r) * 2 + mt) * 1024 + lane * 16) = v;
      }
    }
    STAMP(4);
    const int th = r >> 1, eh = r & 1;
    const int l31 = lane & 31, hh = lane >> 5;
    f32x16 acc[2][2] = {};
    {
      const u32 a_off = eh * 4096 + ((lane >> 4) & 1) * 1024 + hh * 256 + i16 * 16;
      u32 c_off[4];
#pragma unroll
      for (int k4 = 0; k4 < 4; ++k4) c_off[k4] = (th * 64 + l31) * 128 + (((k4 * 2 + hh) ^ ((l31 >> 1) & 7)) << 4);
      bf16x8 A0[2][2], B0[2][2], A1[2][2], B1[2][2];
      auto rd = [&](bf16x8 (*A)[2], bf16x8 (*B)[2], int st, int kh) {
        LAS char* sa = L + blkAB(st) + a_off;
        LAS char* sc = L + blkC(st);
#pragma unroll
        for (int k2 = 0; k2 < 2; ++k2)
#pragma unroll
          for (int e = 0; e < 2; ++e) {
            A[k2][e] = *(const LAS bf16x8*)(sa + kh * 8192 + e * 2048 + k2 * 512);
            B[k2][e] = *(const LAS bf16x8*)(sc + c_off[kh * 2 + k2] + e * 4096);
          }
      };
      auto mm = [&](bf16x8 (*A)[2], bf16x8 (*B)[2]) {
#pragma unroll
        for (int k2 = 0; k2 < 2; ++k2)
#pragma unroll
          for (int e = 0; e < 2; ++e)
#pragma unroll
            for (int t = 0; t < 2; ++t) acc[e][t] = MFMA32(A[k2][e], B[k2][t], acc[e][t]);
      };
      auto sched = [&]() {
#pragma unroll
        for (int i = 0; i < 4; ++i) {
          __builtin_amdgcn_sched_group_barrier(0x008, 1, 0);
          __builtin_amdgcn_sched_group_barrier(0x100, 2, 0);
        }
        __builtin_amdgcn_sched_group_barrier(0x008, 4, 0);
      };
      __builtin_amdgcn_s_setprio(2);
      WAIT_V(0); WAIT_L0(); BARS(0);
      STAMP(5);
      rd(A0, B0, 0, 0);
#pragma unroll 1
      for (int st = 0; st < 15; ++st) {
        rd(A1, B1, st, 1);
        mm(A0, B0);
        sched();
        WAIT_L0(); BARS(st + 1);
        rd(A0, B0, st + 1, 0);
        mm(A1, B1);
        sched();
        SB0();
      }
      rd(A1, B1, 15, 1);
      mm(A0, B0);
      sched();
      WAIT_L0(); BARS(16);
      mm(A1, B1);
    }
    STAMP(6);
    if (r == sg) {
#pragma unroll
      for (int e = 0; e < 2; ++e)
#pragma unroll
        for (int t = 0; t < 2; ++t)
#pragma unroll
          for (int rg = 0; rg < 4; ++rg) {
            f32x4 v = {acc[e][t][4 * rg], acc[e][t][4 * rg + 1], acc[e][t][4 * rg + 2], acc[e][t][4 * rg + 3]};
            *(LAS f32x4*)(L + MISC + ((e * 2 + t) * 4 + rg) * 1024 + lane * 16) = v;
          }
    }
    const u32 pdst = WS_P + ((tile * 4 + r) * 4 + sg) * 16384 + lane * 16;
    auto pstore = [&](int i) {
      if (r != sg) {
        {
          const int e = i >> 2, t = (i >> 1) & 1, h2 = i & 1;
          u32x4 v = {pack2(acc[e][t][8 * h2], acc[e][t][8 * h2 + 1]), pack2(acc[e][t][8 * h2 + 2], acc[e][t][8 * h2 + 3]),
                     pack2(acc[e][t][8 * h2 + 4], acc[e][t][8 * h2 + 5]), pack2(acc[e][t][8 * h2 + 6], acc[e][t][8 * h2 + 7])};
          __builtin_amdgcn_raw_buffer_store_b128(v, ws_rs, pdst + i * 1024, 0, 16);
        }
        if (false)
#pragma unroll
        for (int u = 2 * i; u < 2 * i + 2; ++u) {
          const int e = u >> 3, t = (u >> 2) & 1, rg = u & 3;
          f32x4 v = {acc[e][t][4 * rg], acc[e][t][4 * rg + 1], acc[e][t][4 * rg + 2], acc[e][t][4 * rg + 3]};
          __builtin_amdgcn_raw_buffer_store_b128(__builtin_bit_cast(u32x4, v), ws_rs, pdst + u * 1024, 0, 16);
        }
      }
    };
    STAMP(7);
    const int dh = r & 1, t2 = r >> 1;
    const int d0 = nt2 * 128 + (sg & 1) * 64 + dh * 32 + 4 * hh;
    f32x16 oc = {};
    u32x2 bv[4];
    {
      const int a = i16 >> 2, p = lane & 3;
      const u32 wb = (8 * hh + a) * 128 + (((dh * 2 + ((lane >> 4) & 1)) ^ (a & 2)) * 32) + p * 8;
      const u32 hb = t2 * 2048 + ((lane >> 4) & 1) * 1024 + hh * 256 + i16 * 16;
      bf16x8 A0[2], B0[2], A1[2], B1[2];
      auto rd = [&](bf16x8* A, bf16x8* B, int j, int kk) {
        LAS char* sw2 = L + blkAB(j) + wb;
        LAS char* sh = L + blkC(j) + hb;
#pragma unroll
        for (int k2 = 0; k2 < 2; ++k2) {
          s16x4 lo = __builtin_amdgcn_ds_read_tr16_b64_v4i16((LAS s16x4*)(sw2 + kk * 4096 + k2 * 2048));
          s16x4 hi = __builtin_amdgcn_ds_read_tr16_b64_v4i16((LAS s16x4*)(sw2 + kk * 4096 + k2 * 2048 + 512));
          A[k2] = __builtin_shufflevector(lo, hi, 0, 1, 2, 3, 4, 5, 6, 7);
          B[k2] = *(const LAS bf16x8*)(sh + kk * 4096 + k2 * 512);
        }
      };
      auto mm = [&](bf16x8* A, bf16x8* B) {
        oc = MFMA32(A[0], B[0], oc);
        oc = MFMA32(A[1], B[1], oc);
      };
      auto sched = [&]() {
        __builtin_amdgcn_sched_group_barrier(0x100, 3, 0);
        __builtin_amdgcn_sched_group_barrier(0x008, 1, 0);
        __builtin_amdgcn_sched_group_barrier(0x100, 3, 0);
        __builtin_amdgcn_sched_group_barrier(0x008, 1, 0);
        SB0();
      };
      rd(A0, B0, 0, 0);
      SB0();
#pragma unroll
      for (int j = 0; j < 2; ++j) {
        rd(A1, B1, j, 1); mm(A0, B0); sched(); pstore(4 * j);
        rd(A0, B0, j, 2); mm(A1, B1); sched(); pstore(4 * j + 1);
        rd(A1, B1, j, 3); mm(A0, B0); sched(); pstore(4 * j + 2);
        WAIT_L0(); BARS(17 + j);
        rd(A0, B0, j + 1, 0); mm(A1, B1); sched(); pstore(4 * j + 3);
      }
#pragma unroll
      for (int rg = 0; rg < 4; ++rg) {
        asm volatile("global_load_dwordx2 %0, %1, off" : "=v"(bv[rg]) : "v"(b2 + d0 + rg * 8) : "memory");
      }
#pragma unroll 1
      for (int j = 2; j < 10; ++j) {
        rd(A1, B1, j, 1); mm(A0, B0); sched();
        rd(A0, B0, j, 2); mm(A1, B1); sched();
        rd(A1, B1, j, 3); mm(A0, B0); sched();
        if (j == 3) {
          WAIT_V(0);
          if (lane == 0 && r != sg) __hip_atomic_store((gu64*)(ws + WS_FP) + (tile * 4 + r) * 8 + sg, tag, RLX_AGENT);
        }
        WAIT_L0(); BARS(17 + j);
        rd(A0, B0, j + 1, 0); mm(A1, B1); sched();
      }
      rd(A1, B1, 10, 1); mm(A0, B0); sched();
      rd(A0, B0, 10, 2); mm(A1, B1); sched();
      mm(A0, B0);
    }
    STAMP(8);
    WAIT_V(0);
#pragma unroll
    for (int rg = 0; rg < 4; ++rg) asm volatile("" : "+v"(bv[rg]));
    float gate[16];
#pragma unroll
    for (int rg = 0; rg < 4; ++rg) {
      const float bb[4] = {lo_f(bv[rg][0]), hi_f(bv[rg][0]), lo_f(bv[rg][1]), hi_f(bv[rg][1])};
#pragma unroll
      for (int j = 0; j < 4; ++j) {
        const float o2 = oc[4 * rg + j] + bb[j];
        gate[4 * rg + j] = o2 * sigm(o2);
      }
    }
#pragma unroll
    for (int j = 0; j < 16; j += 4) asm volatile("" : "+v"(gate[j]), "+v"(gate[j + 1]), "+v"(gate[j + 2]), "+v"(gate[j + 3]));
    BARS(27);
#pragma unroll
    for (int rg = 0; rg < 4; ++rg) {
      const u32 o = ((dh * 2 + t2) * 4 + rg) * 1024 + lane * 16;
      const f32x4 y0 = *(const LAS f32x4*)(L + MISC + o);
      const u32 ob = ((dh * 2 + t2) * 2 + (rg >> 1)) * 1024 + lane * 16 + (rg & 1) * 8;
      const u32x2 q1 = *(const LAS u32x2*)(L + 8 * BLK + ob), q2 = *(const LAS u32x2*)(L + 3 * BLK + ob), q3 = *(const LAS u32x2*)(L + 3 * BLK + 8192 + ob);
      LAS u32x2* xp = (LAS u32x2*)(L + 8 * BLK + 8192 + (t2 * 32 + l31) * 128 + (((dh * 4 + rg) ^ ((l31 >> 1) & 7)) << 4) + hh * 8);
      const u32x2 xv = *xp;
      const float ya[4] = {y0[0] + lo_f(q1[0]), y0[1] + hi_f(q1[0]), y0[2] + lo_f(q1[1]), y0[3] + hi_f(q1[1])};
      const float yb[4] = {lo_f(q2[0]) + lo_f(q3[0]), hi_f(q2[0]) + hi_f(q3[0]), lo_f(q2[1]) + lo_f(q3[1]), hi_f(q2[1]) + hi_f(q3[1])};
      const float xx[4] = {lo_f(xv[0]), hi_f(xv[0]), lo_f(xv[1]), hi_f(xv[1])};
      float res[4];
#pragma unroll
      for (int j = 0; j < 4; ++j) res[j] = xx[j] + (ya[j] + yb[j]) * gate[4 * rg + j];
      u32x2 ov = {pack2(res[0], res[1]), pack2(res[2], res[3])};
      *xp = ov;
    }
    WAIT_L0();
    BAR();
    STAMP(9);
  }
  {
    const int row = tid >> 3, ch = tid & 7;
    const u32x4 v = *(const LAS u32x4*)(L + 8 * BLK + 8192 + row * 128 + ch * 16);
    *(u32x4*)(out + (size_t)(stile * 128 + (sg >> 1) * 64 + row) * 512 + nt2 * 128 + (sg & 1) * 64 + ((ch ^ ((row >> 1) & 7)) << 3)) = v;
  }
  if (bid == 0 && wave == 4) {
    for (int k = 0; k < 8; ++k) poll_flags<4>((gu64*)(ws + WS_FAB) + k * 256 + lane, tag);
    if (lane == 0) __hip_atomic_store((gu64*)(ws + WS_EPOCH), tag, RLX_AGENT);
  }
  STAMP_FLUSH();
}

extern "C" void kernel_launch(void* const* d_in, const int* in_sizes, int n_in, void* d_out, int out_size, void* d_ws, size_t ws_size,
                              hipStream_t stream) {
  fused_k<<<256, 512, 0, stream>>>((const u16*)d_in[0], (const u16*)d_in[1], (const u16*)d_in[2], (const u16*)d_in[3], (const u16*)d_in[4],
                                   (const u16*)d_in[6], (const u16*)d_in[7], (const u16*)d_in[8], (const u16*)d_in[9], (u16*)d_out,
                                   (char*)d_ws);
}
```

```cpp
#include <hip/hip_runtime.h>
#include <hip/hip_bf16.h>
#include <stdint.h>

typedef unsigned short u16;
typedef unsigned int u32;
typedef unsigned long long u64;
typedef __attribute__((ext_vector_type(8))) short bf16x8;
typedef __attribute__((ext_vector_type(4))) short s16x4;
typedef __attribute__((ext_vector_type(2))) __bf16 bf2;
typedef __attribute__((ext_vector_type(4))) float f32x4;
typedef __attribute__((ext_vector_type(2))) float f32x2;
typedef __attribute__((ext_vector_type(16))) float f32x16;
typedef __attribute__((ext_vector_type(4))) unsigned u32x4;
typedef __attribute__((ext_vector_type(2))) unsigned u32x2;
#define LAS __attribute__((address_space(3)))
#define GAS __attribute__((address_space(1)))
typedef GAS u64 gu64;

#define PBF16 1
constexpr u32 WS_EPOCH = 0;
constexpr u32 WS_FAB = 4096;
constexpr u32 WS_FH = WS_FAB + 16384;
constexpr u32 WS_FP = WS_FH + 16384;
constexpr u32 WS_AB = 1u << 20;
constexpr u32 WS_H = WS_AB + (4u << 20);
constexpr u32 WS_P = WS_H + (6u << 20);
constexpr u32 WS_DBG = WS_P + (16u << 20);
constexpr u32 WS_END = WS_DBG + (2u << 20);

constexpr u32 BLK = 16384;
constexpr u32 MISC = 9 * BLK;
constexpr u32 M_STAT = MISC;
constexpr u32 M_WS = MISC + 1024;
constexpr u32 M_CS = MISC + 2048;
constexpr u32 M_DUMMY = MISC + 4096;

#define RLX_AGENT __ATOMIC_RELAXED, __HIP_MEMORY_SCOPE_AGENT
#define WAIT_V(n) asm volatile("s_waitcnt vmcnt(" #n ")" ::: "memory")
#define WAIT_L0() asm volatile("s_waitcnt lgkmcnt(0)" ::: "memory")
#define BAR() __builtin_amdgcn_s_barrier()
#define SB0() __builtin_amdgcn_sched_barrier(0)
#define RFL(x) ((u32)__builtin_amdgcn_readfirstlane((u32)(x)))
#define BARS(i) BAR()
constexpr u32 SPIN_CAP = 1u << 20;

#define STAMP(i) do {} while (0)
#define STAMPD(i) do {} while (0)
#define STAMP_DECL
#define STAMP_FLUSH() do {} while (0)

__device__ __forceinline__ float rbf(float v) { return (float)(__bf16)v; }
__device__ __forceinline__ u32 pack2(float a, float b) { bf2 v = {(__bf16)a, (__bf16)b}; return __builtin_bit_cast(u32, v); }
__device__ __forceinline__ float lo_f(u32 v) { return __builtin_bit_cast(float, v << 16); }
__device__ __forceinline__ float hi_f(u32 v) { return __builtin_bit_cast(float, v & 0xffff0000u); }
__device__ __forceinline__ float sigm(float z) {
  return __builtin_amdgcn_rcpf(1.0f + __builtin_amdgcn_exp2f(-1.4426950408889634f * z));
}
__device__ __forceinline__ float gelu_t(float u) {
  const float z = u * (-2.3022081983f - 0.1029432398f * u * u);
  return u * __builtin_amdgcn_rcpf(1.0f + __builtin_amdgcn_exp2f(z));
}
__device__ __forceinline__ u32x4 mk_rs(const void* p, u32 bytes) {
  const u64 a = (u64)p;
  u32x4 r = {RFL(a), RFL((u32)(a >> 32) & 0xffffu), bytes, 0x00020000u};
  return r;
}
#define DMA4M(MOD, rs, d, v0, v1, v2, v3, so) do { u32 keep_; \
  asm volatile("s_nop 4\n\ts_mov_b32 %0, m0\n\ts_mov_b32 m0, %1\n\ts_nop 0\n\t" \
               "buffer_load_dwordx4 %2, %6, %7 offen " MOD " lds\n\t" \
               "buffer_load_dwordx4 %3, %6, %7 offen offset:1024 " MOD " lds\n\t" \
               "buffer_load_dwordx4 %4, %6, %7 offen offset:2048 " MOD " lds\n\t" \
               "buffer_load_dwordx4 %5, %6, %7 offen offset:3072 " MOD " lds\n\t" \
               "s_mov_b32 m0, %0" \
               : "=&s"(keep_) : "s"(RFL(d)), "v"(v0), "v"(v1), "v"(v2), "v"(v3), "s"(rs), "s"(RFL(so)) : "memory"); } while (0)
#define DMA4(rs, d, v0, v1, v2, v3, so) DMA4M("", rs, d, v0, v1, v2, v3, so)
#define DMA4S(rs, d, v0, v1, v2, v3, so) DMA4M("sc1", rs, d, v0, v1, v2, v3, so)
#define DMA2(rs, d, v0, v1, so) do { u32 keep_; \
  asm volatile("s_nop 4\n\ts_mov_b32 %0, m0\n\ts_mov_b32 m0, %1\n\ts_nop 0\n\t" \
               "buffer_load_dwordx4 %2, %4, %5 offen lds\n\t" \
               "buffer_load_dwordx4 %3, %4, %5 offen offset:1024 lds\n\t" \
               "s_mov_b32 m0, %0" \
               : "=&s"(keep_) : "s"(RFL(d)), "v"(v0), "v"(v1), "s"(rs), "s"(RFL(so)) : "memory"); } while (0)
#define TOUCH(rs, d, v0, so) do { u32 keep_; \
  asm volatile("s_nop 4\n\ts_mov_b32 %0, m0\n\ts_mov_b32 m0, %1\n\ts_nop 0\n\t" \
               "buffer_load_dword %2, %3, %4 offen lds\n\t" \
               "s_mov_b32 m0, %0" \
               : "=&s"(keep_) : "s"(RFL(d)), "v"(v0), "s"(rs), "s"(RFL(so)) : "memory"); } while (0)
#define TOUCHG(d, p) do { u32 keep_; \
  asm volatile("s_mov_b32 %0, m0\n\ts_mov_b32 m0, %1\n\ts_nop 0\n\t" \
               "global_load_lds_dword %2, off\n\t" \
               "s_mov_b32 m0, %0" \
               : "=&s"(keep_) : "s"(RFL(d)), "v"(p) : "memory"); } while (0)
#define MFMA(a, b, c) __builtin_amdgcn_mfma_f32_16x16x32_bf16(a, b, c, 0, 0, 0)
#define MFMA32(a, b, c) __builtin_amdgcn_mfma_f32_32x32x16_bf16(a, b, c, 0, 0, 0)

template <int NPER>
__device__ __forceinline__ void poll_flags(gu64* f, u64 tag, bool skip = false) {
  for (u32 sp = 0; sp < SPIN_CAP; ++sp) {
    bool ok = true;
#pragma unroll
    for (int k = 0; k < NPER; ++k) {
      u64 v = __hip_atomic_load(f + k * 64, RLX_AGENT);
      ok &= (v == tag);
    }
    if (__all(ok || skip)) break;
    __builtin_amdgcn_s_sleep(1);
  }
  asm volatile("" ::: "memory");
}
#define POLL_ISSUE(val, ptr) asm volatile("global_load_dwordx2 %0, %1, off sc1" : "=v"(val) : "v"(ptr) : "memory")

__global__ __launch_bounds__(512, 2) void fused_k(const u16* __restrict__ x, const u16* __restrict__ Cr, const u16* __restrict__ Ci,
                                                   const u16* __restrict__ Wr, const u16* __restrict__ Wi, const u16* __restrict__ w1,
                                                   const u16* __restrict__ b1, const u16* __restrict__ w2, const u16* __restrict__ b2,
                                                   u16* __restrict__ out, char* __restrict__ ws) {
  __shared__ __attribute__((aligned(1024))) char smem[163840];
  const int tid = threadIdx.x, lane = tid & 63;
  const int wave = __builtin_amdgcn_readfirstlane(tid >> 6);
  const int bid = blockIdx.x, xcd = bid & 7, idx = bid >> 3;
  const int stile = (xcd & 1) * 8 + (idx & 7);
  const int sg = xcd >> 1;
  const int nt2 = idx >> 3;
  const int cg = sg * 4 + nt2;
  const int g = wave >> 2, r = wave & 3;
  const int i16 = lane & 15, q = lane >> 4;
  LAS char* L = (LAS char*)smem;
  const u32 lb = (u32)(size_t)L;
  STAMP_DECL;
  STAMP(0);

  const __amdgpu_buffer_rsrc_t ws_rs = __builtin_amdgcn_make_buffer_rsrc((void*)ws, 0, WS_END, 0x00020000);
  const u32x4 ws_r4 = mk_rs(ws, WS_END);
  const u32x4 c_rs = mk_rs((sg < 2 ? Cr : Ci) + (size_t)stile * 128 * 2048 + (sg & 1) * 1024, 128 * 4096);

  const int nh = cg < 11 ? 3 : 2;
  const int hu0 = cg < 11 ? 3 * cg : 33 + 2 * (cg - 11);
  const int hu2 = cg < 11 ? hu0 + 2 : hu0;
  u32x2 bvA[2], bvC[2];
#pragma unroll
  for (int ft = 0; ft < 2; ++ft) {
    asm volatile("global_load_dwordx2 %0, %1, off" : "=v"(bvA[ft]) : "v"(b1 + (hu0 + g) * 32 + ft * 16 + 4 * q) : "memory");
    asm volatile("global_load_dwordx2 %0, %1, off" : "=v"(bvC[ft]) : "v"(b1 + hu2 * 32 + ft * 16 + 4 * q) : "memory");
  }
  u64 tag;
  POLL_ISSUE(tag, (gu64*)(ws + WS_EPOCH));

  const u32x4 x_rs = mk_rs(x + (size_t)stile * 128 * 512, 131072);
  const u32x4 w_rs = mk_rs((cg < 8 ? Wr : Wi) + (size_t)(cg & 7) * 64 * 512, 65536);
  int xq[4], wq[2];
#pragma unroll
  for (int ii = 0; ii < 4; ++ii) xq[ii] = (wave * 16 + 4 * ii + q) * 1024 + ((i16 ^ (4 * ii + q)) << 4) - ii * 1024;
#pragma unroll
  for (int ii = 0; ii < 2; ++ii) wq[ii] = (wave * 8 + 4 * ii + q) * 1024 + ((i16 ^ ((wave * 8 + 4 * ii + q) & 15)) << 4) - ii * 1024;
  {
#pragma unroll
    for (int kq = 0; kq < 3; ++kq) {
      DMA4(x_rs, lb + kq * 3 * BLK + wave * 4096, xq[0], xq[1], xq[2], xq[3], kq * 256);
      DMA2(w_rs, lb + kq * 3 * BLK + 2 * BLK + wave * 2048, wq[0], wq[1], kq * 256);
      if (kq < 2) BAR();
    }
  }
  STAMP(10);

  const u32x4 w1_rs = mk_rs(w1, 512 * 1376 * 2);
  constexpr int W1ST = 16 * 2752 - 1024;
  const int w1vo = (lane >> 2) * 2752 + (((((lane & 3) >> 1) ^ (lane >> 5)) & 1) << 5) + ((lane & 1) << 4);
  auto issue_h = [&](int unit, u32 d0, u32 d1) {
    DMA4(w1_rs, lb + (g ? d1 : d0) + r * 4096, w1vo, w1vo + W1ST, w1vo + 2 * W1ST, w1vo + 3 * W1ST, wave * 64 * 2752 + unit * 64);
  };

  const bf16x8 ones = {0x3f80, 0x3f80, 0x3f80, 0x3f80, 0x3f80, 0x3f80, 0x3f80, 0x3f80};
  bf16x8 xf[2][16];
  f32x4 accA[2][2] = {};
  f32x4 sS = {}, sQ = {}, wsA[2] = {};
  {
    u32 sw[4];
#pragma unroll
    for (int k3 = 0; k3 < 4; ++k3) sw[k3] = i16 * 256 + (((k3 * 4 + q) ^ i16) << 4);
    const u32 xo0 = r * 8192 + g * 4096, xo1 = r * 8192 + (g ^ 1) * 4096, wo = 2 * BLK + g * 8192;
    bf16x8 wf[2][4][2];
#pragma unroll
    for (int kq = 0; kq < 5; ++kq) {
      if (kq < 4) {
        if (kq == 0) WAIT_V(12); else if (kq == 1) WAIT_V(6); else if (kq == 2) WAIT_V(6); else WAIT_V(4);
        if (kq == 0) { asm volatile("" : "+v"(bvA[0]), "+v"(bvA[1]), "+v"(bvC[0]), "+v"(bvC[1]), "+v"(tag)); tag += 1ull; }
        WAIT_L0();
        BAR();
        if (kq == 0) STAMP(1);
        if (kq == 1) STAMP(11);
        if (kq == 2) STAMP(12);
        if (kq == 3) STAMP(13);
      }
      {
        const u32 qb = (kq % 3) * 3 * BLK;
        const int kp = kq - 1;
#pragma unroll
        for (int k3 = 0; k3 < 4; ++k3) {
          if (kq < 4) {
            xf[0][kq * 4 + k3] = *(const LAS bf16x8*)(L + qb + xo0 + sw[k3]);
            xf[1][kq * 4 + k3] = *(const LAS bf16x8*)(L + qb + xo1 + sw[k3]);
#pragma unroll
            for (int nt = 0; nt < 2; ++nt) wf[kq & 1][k3][nt] = *(const LAS bf16x8*)(L + qb + wo + nt * 4096 + sw[k3]);
          }
          if (kq > 0) {
            const int ks = kp * 4 + k3;
#pragma unroll
            for (int nt = 0; nt < 2; ++nt) {
              accA[nt][0] = MFMA(xf[0][ks], wf[kp & 1][k3][nt], accA[nt][0]);
              accA[nt][1] = MFMA(xf[1][ks], wf[kp & 1][k3][nt], accA[nt][1]);
            }
            sS = MFMA(ones, xf[0][ks], sS);
            sQ = MFMA(xf[0][ks], xf[0][ks], sQ);
            if (kq < 4) {
#pragma unroll
              for (int i = 0; i < 4; ++i) {
                __builtin_amdgcn_sched_group_barrier(0x008, 1, 0);
                __builtin_amdgcn_sched_group_barrier(0x100, 1, 0);
              }
              __builtin_amdgcn_sched_group_barrier(0x008, 2, 0);
            }
          }
          SB0();
          if (kq == 1 && k3 == 0) DMA4(x_rs, lb + wave * 4096, xq[0], xq[1], xq[2], xq[3], 768);
          if (kq == 1 && k3 == 1) DMA2(w_rs, lb + 2 * BLK + wave * 2048, wq[0], wq[1], 768);
          if (kq == 2 && k3 == 0) issue_h(hu0, 3 * BLK, 4 * BLK);
          if (kq == 3 && k3 == 0) issue_h(hu0 + 1, 6 * BLK, 7 * BLK);
          if (kq == 3 && k3 == 1) issue_h(hu2, 5 * BLK, 8 * BLK);
          SB0();
        }
      }
      if (kq > 0) {
        const int kp = kq - 1;
        if (r == kp) {
#pragma unroll
          for (int k3 = 0; k3 < 4; ++k3)
#pragma unroll
            for (int nt = 0; nt < 2; ++nt) wsA[nt] = MFMA(ones, wf[kp & 1][k3][nt], wsA[nt]);
        }
        SB0();
      }
    }
  }
  {
    const int j = i16 & 3;
    float d = j == 0 ? sQ[0] : j == 1 ? sQ[1] : j == 2 ? sQ[2] : sQ[3];
    if (q == (i16 >> 2)) { f32x2 v = {sS[0], d}; *(LAS f32x2*)(L + M_STAT + (r * 32 + g * 16 + i16) * 8) = v; }
    if (q == 0) {
      *(LAS float*)(L + M_WS + ((g * 4 + r) * 32 + i16) * 4) = wsA[0][0];
      *(LAS float*)(L + M_WS + ((g * 4 + r) * 32 + 16 + i16) * 4) = wsA[1][0];
    }
  }
  WAIT_V(4);
  WAIT_L0();
  BAR();
  STAMP(2);

  int cvo[4];
#pragma unroll
  for (int ii = 0; ii < 4; ++ii)
    cvo[ii] = ((r * 4 + ii) * 8 + (lane >> 3)) * 4096 + (((lane & 7) ^ (((ii & 1) * 4 + (lane >> 4)) & 7)) << 4) - ii * 1024;
  const int lv = lane * 16;
  auto blkC = [&](int st) -> u32 { return (u32)(st & 3) * BLK; };
  auto blkAB = [&](int st) -> u32 { return ((0x5764u >> ((st & 3) * 4)) & 15u) * BLK; };
  const int rot = 2 * (stile & 7) + (nt2 & 1) + 8 * (nt2 >> 1);
  auto issue_c = [&](int st) { DMA4(c_rs, lb + blkC(st) + r * 4096, cvo[0], cvo[1], cvo[2], cvo[3], ((st + rot) & 15) * 128); };
  const u32 ab_src = WS_AB + (((sg >> 1) * 4 + nt2) * 64 + (sg & 1) * 32) * 8192 + r * 4096;
  auto issue_ab = [&](int st) { DMA4S(ws_r4, lb + blkAB(st) + r * 4096, lv, lv, lv, lv, ab_src + ((st + rot) & 15) * 16384); };

  const int c4 = (q & 1) * 2 + (q >> 1);
  float mH[2], rH[2];
  float mA[2], rA[2], mrow[2][4], rrow[2][4], wsv[2];
  auto epiA = [&](int ks) {
    if (ks == 0) {
#pragma unroll
      for (int mt = 0; mt < 2; ++mt) {
        const f32x2 sv = *(const LAS f32x2*)(L + M_STAT + (r * 32 + (mt ^ g) * 16 + i16) * 8);
        const float mean = sv[0] * (1.f / 512.f);
        const float var = fmaxf(sv[1] * (1.f / 512.f) - mean * mean, 0.f);
        const float mb = rbf(mean);
        const float den = rbf(__builtin_amdgcn_sqrtf(rbf(rbf(var) + 1e-5f)));
        const float inv = __builtin_amdgcn_rcpf(den);
        const float m2 = rbf((mean - mb) * inv);
        const float den2 = rbf(__builtin_amdgcn_sqrtf(rbf(rbf(var * inv * inv) + 1e-5f)));
        mA[mt] = mb; rA[mt] = cg < 8 ? inv : -inv;
        rH[mt] = inv * __builtin_amdgcn_rcpf(den2); mH[mt] = (mb + m2 * den) * rH[mt];
      }
    } else if (ks == 1) {
#pragma unroll
      for (int mt = 0; mt < 2; ++mt)
#pragma unroll
        for (int j = 0; j < 4; ++j) { mrow[mt][j] = __shfl(mA[mt], 4 * q + j); rrow[mt][j] = __shfl(rA[mt], 4 * q + j); }
#pragma unroll
      for (int nt = 0; nt < 2; ++nt) {
        wsv[nt] = 0.f;
#pragma unroll
        for (int rr = 0; rr < 4; ++rr) wsv[nt] += *(const LAS float*)(L + M_WS + ((g * 4 + rr) * 32 + nt * 16 + i16) * 4);
      }
    } else if (ks == 3 || ks == 5) {
      const int nt = ks == 5;
      const u32 dst = WS_AB + ((((cg >> 3) * 4 + ((cg & 7) >> 1)) * 64 + stile * 4 + r) * 8 + (cg & 1) * 4 + g * 2) * 1024 + ((c4 ^ (g * 2)) * 16 + i16) * 16;
      u32 p[2][2];
#pragma unroll
      for (int mt = 0; mt < 2; ++mt) {
        float v[4];
#pragma unroll
        for (int j = 0; j < 4; ++j) v[j] = (accA[nt][mt][j] - mrow[mt][j] * wsv[nt]) * rrow[mt][j];
        p[mt][0] = pack2(v[0], v[1]); p[mt][1] = pack2(v[2], v[3]);
      }
      auto s0 = __builtin_amdgcn_permlane16_swap(p[0][0], p[1][0], false, false);
      auto s1 = __builtin_amdgcn_permlane16_swap(p[0][1], p[1][1], false, false);
      u32x4 sv = {s0[0], s1[0], s0[1], s1[1]};
      __builtin_amdgcn_raw_buffer_store_b128(sv, ws_rs, dst + nt * 1024, 0, 16);
    }
  };
  epiA(0); epiA(1); epiA(3); epiA(5);
  SB0();
  auto sideB = [&](int ks) {
    if (ks == 6) {
      WAIT_V(0);
      if (lane == 0)
        __hip_atomic_store((gu64*)(ws + WS_FAB) + (((((cg >> 3) * 4 + ((cg & 7) >> 1)) * 16 + stile) * 2 + (cg & 1)) * 8 + wave), tag, RLX_AGENT);
    } else if (ks == 8 || ks == 10 || ks == 12) {
      if (g == 0) issue_c((ks - 8) >> 1);
    }
  };

  auto h_mma = [&](f32x4 (&acc)[2][2], u32 d0, u32 d1, int slot, auto&& side) {
    const int a = i16 >> 2, p = lane & 3;
    u32 tb[2][2];
#pragma unroll
    for (int ft = 0; ft < 2; ++ft) {
      const u32 o = (8 * q + a) * 64 + ((ft ^ (q & 1)) * 32) + p * 8;
      tb[0][ft] = d0 + o; tb[1][ft] = d1 + o;
      acc[ft][0] = (f32x4){0.f, 0.f, 0.f, 0.f}; acc[ft][1] = (f32x4){0.f, 0.f, 0.f, 0.f};
    }
    {
      f32x4 cs[2] = {};
      const u32 cb = (r < 2 ? d0 : d1) + (r & 1) * 8192 - d0;
#pragma unroll
      for (int k = 0; k < 4; ++k)
#pragma unroll
        for (int ft = 0; ft < 2; ++ft) {
          s16x4 lo = __builtin_amdgcn_ds_read_tr16_b64_v4i16((LAS s16x4*)(L + tb[0][ft] + cb + k * 2048));
          s16x4 hi = __builtin_amdgcn_ds_read_tr16_b64_v4i16((LAS s16x4*)(L + tb[0][ft] + cb + k * 2048 + 256));
          cs[ft] = MFMA(__builtin_shufflevector(lo, hi, 0, 1, 2, 3, 4, 5, 6, 7), ones, cs[ft]);
        }
      if (i16 == 0) {
        *(LAS f32x4*)(L + M_CS + ((slot * 4 + r) * 32 + 4 * q) * 4) = cs[0];
        *(LAS f32x4*)(L + M_CS + ((slot * 4 + r) * 32 + 16 + 4 * q) * 4) = cs[1];
      }
    }
    bf16x8 wf[3][2];
    auto ldw = [&](int ks) {
#pragma unroll
      for (int ft = 0; ft < 2; ++ft) {
        s16x4 lo = __builtin_amdgcn_ds_read_tr16_b64_v4i16((LAS s16x4*)(L + tb[ks >> 3][ft] + (ks & 7) * 2048));
        s16x4 hi = __builtin_amdgcn_ds_read_tr16_b64_v4i16((LAS s16x4*)(L + tb[ks >> 3][ft] + (ks & 7) * 2048 + 256));
        wf[ks % 3][ft] = __builtin_shufflevector(lo, hi, 0, 1, 2, 3, 4, 5, 6, 7);
      }
    };
    ldw(0); ldw(1);
    SB0();
#pragma unroll
    for (int ks = 0; ks < 16; ++ks) {
      if (ks + 2 < 16) ldw(ks + 2);
#pragma unroll
      for (int ft = 0; ft < 2; ++ft) {
        acc[ft][0] = MFMA(wf[ks % 3][ft], xf[0][ks], acc[ft][0]);
        acc[ft][1] = MFMA(wf[ks % 3][ft], xf[1][ks], acc[ft][1]);
      }
      side(ks);
      SB0();
    }
  };
  auto preH = [&](f32x4 (&acc)[2][2], int slot, const u32x2* bv, u32 (&pa)[2][2][2]) {
#pragma unroll
    for (int ft = 0; ft < 2; ++ft) {
      f32x4 ecs = *(const LAS f32x4*)(L + M_CS + ((slot * 4 + 0) * 32 + ft * 16 + 4 * q) * 4);
#pragma unroll
      for (int rr = 1; rr < 4; ++rr) ecs += *(const LAS f32x4*)(L + M_CS + ((slot * 4 + rr) * 32 + ft * 16 + 4 * q) * 4);
      const float bj[4] = {lo_f(bv[ft][0]), hi_f(bv[ft][0]), lo_f(bv[ft][1]), hi_f(bv[ft][1])};
#pragma unroll
      for (int mt = 0; mt < 2; ++mt) {
        float v[4];
#pragma unroll
        for (int jj = 0; jj < 4; ++jj) v[jj] = acc[ft][mt][jj] * rH[mt] + (bj[jj] - mH[mt] * ecs[jj]);
        pa[mt][ft][0] = pack2(v[0], v[1]); pa[mt][ft][1] = pack2(v[2], v[3]);
      }
    }
  };
  auto actH = [&](u32 p00, u32 p01, u32 p10, u32 p11, int unit, int trow16) {
    const u32 dst = WS_H + (((stile * 2 + (r >> 1)) * 43 + unit) * 4 + (r & 1) * 2 + trow16) * 1024 + (c4 * 16 + i16) * 16;
    const u32 k00 = pack2(gelu_t(lo_f(p00)), gelu_t(hi_f(p00))), k01 = pack2(gelu_t(lo_f(p01)), gelu_t(hi_f(p01)));
    const u32 k10 = pack2(gelu_t(lo_f(p10)), gelu_t(hi_f(p10))), k11 = pack2(gelu_t(lo_f(p11)), gelu_t(hi_f(p11)));
    auto s0 = __builtin_amdgcn_permlane16_swap(k00, k10, false, false);
    auto s1 = __builtin_amdgcn_permlane16_swap(k01, k11, false, false);
    u32x4 sv = {s0[0], s1[0], s0[1], s1[1]};
    __builtin_amdgcn_raw_buffer_store_b128(sv, ws_rs, dst, 0, 16);
  };
  auto act2 = [&](u32 p) -> u32 { return pack2(gelu_t(lo_f(p)), gelu_t(hi_f(p))); };
  auto actH_fin = [&](u32 k00, u32 k01, u32 p10, u32 p11, int unit, int trow16) {
    const u32 dst = WS_H + (((stile * 2 + (r >> 1)) * 43 + unit) * 4 + (r & 1) * 2 + trow16) * 1024 + (c4 * 16 + i16) * 16;
    auto s0 = __builtin_amdgcn_permlane16_swap(k00, act2(p10), false, false);
    auto s1 = __builtin_amdgcn_permlane16_swap(k01, act2(p11), false, false);
    u32x4 sv = {s0[0], s1[0], s0[1], s1[1]};
    __builtin_amdgcn_raw_buffer_store_b128(sv, ws_rs, dst, 0, 16);
  };
  f32x4 accB[2][2], accC[2][2];
  h_mma(accB, (3 + 3 * g) * BLK, (4 + 3 * g) * BLK, g, sideB);
  STAMP(14);
  WAIT_L0();
  BAR();
  STAMP(3);

  const int tile = stile * 4 + nt2;
  if (g == 1) {
    u32 pa[2][2][2];
    preH(accB, 1, bvA, pa);
    poll_flags<2>((gu64*)(ws + WS_FAB) + (((sg >> 1) * 4 + nt2) * 16 + (sg & 1) * 8) * 16 + lane, tag);
    STAMP(4);
    issue_ab(0); issue_ab(1); issue_ab(2); issue_c(3);
    const u32 h_src = WS_H + (stile * 2 + (sg >> 1)) * 43 * 4096;
    const u32x4 w2_rs = mk_rs(w2, 1376 * 512 * 2);
    int wvo[4];
#pragma unroll
    for (int ii = 0; ii < 4; ++ii) {
      const int c = lane & 7, f = ((lane >> 4) & 1) << 1;
      wvo[ii] = (ii * 8 + (lane >> 3)) * 1024 + (nt2 * 128 + (sg & 1) * 64) * 2 + ((((c >> 1) ^ f) << 5) | ((c & 1) << 4)) - ii * 1024;
    }
    const int orot = stile + nt2;
    auto issue_o = [&](int j) {
      int unit;
      if (j < 4) {
        const int c = ((j + orot) & 3) * 4 + r;
        unit = c < 11 ? 3 * c + 1 : 2 * c + 12;
      } else {
        int jj = j - 4 + (orot & 3) + ((orot >> 2) & 1); jj = jj >= 6 ? jj - 6 : jj; jj = j == 10 ? 6 : jj;
        const int t = min(jj * 4 + r, 26);
        unit = t < 22 ? 3 * (t >> 1) + 2 * (t & 1) : 2 * t - 11;
      }
      DMA4S(ws_r4, lb + blkC(j) + r * 4096, lv, lv, lv, lv, h_src + unit * 4096);
      DMA4(w2_rs, lb + blkAB(j) + r * 4096, wvo[0], wvo[1], wvo[2], wvo[3], unit * 32768);
    };
    BAR();
    issue_ab(3);
    actH(pa[0][0][0], pa[0][0][1], pa[0][1][0], pa[0][1][1], hu0 + 1, 1);
    actH(pa[1][0][0], pa[1][0][1], pa[1][1][0], pa[1][1][1], hu0 + 1, 0);
    WAIT_V(18); BARS(0);
    STAMP(5);
    u32x4 pk_;
    u32 ka, kb;
    auto half_a = [&](int u, int mt) {
      pk_ = *(const LAS u32x4*)(L + 8 * BLK + ((u * 4 + r) * 2 + mt) * 1024 + lane * 16);
      ka = act2(pk_[0]); kb = act2(pk_[1]);
      asm volatile("" : "+v"(ka), "+v"(kb));
    };
    auto half_b = [&](int mt, int unit) { actH_fin(ka, kb, pk_[2], pk_[3], unit, mt); };
    WAIT_V(14); BARS(1); issue_c(4); issue_ab(4);
    half_a(0, 0);
    WAIT_V(8); BARS(2);
    if (lane == 0) __hip_atomic_store((gu64*)(ws + WS_FH) + ((stile * 4 + r) * 16 + cg), tag, RLX_AGENT);
    issue_c(5); issue_ab(5);
    half_b(0, hu0);
    WAIT_V(16); BARS(3); issue_c(6); issue_ab(6);
    half_a(0, 1);
    WAIT_V(16); BARS(4); issue_c(7); issue_ab(7);
    half_b(1, hu0);
    WAIT_V(16); BARS(5); issue_c(8); issue_ab(8);
    if (nh == 3) half_a(1, 0);
    WAIT_V(16); BARS(6); issue_c(9); issue_ab(9);
    if (nh == 3) half_b(0, hu2);
    WAIT_V(16); BARS(7); issue_c(10); issue_ab(10);
    if (nh == 3) half_a(1, 1);
    WAIT_V(16); BARS(8); issue_c(11); issue_ab(11);
    if (nh == 3) half_b(1, hu2);
    WAIT_V(16); BARS(9); issue_c(12); issue_ab(12);
    {
      const int row = (stile & 7) * 172 + min(r * 64 + lane, 171);
      TOUCH(w2_rs, lb + 8 * BLK + r * 256, row * 1024 + (nt2 * 128 + (sg & 1) * 64) * 2, 0);
    }
    gu64* fhp = (gu64*)(ws + WS_FH) + (stile * 4 + 2 * (sg >> 1)) * 16 + (lane & 31);
    u64 pv;
    WAIT_V(8); BARS(10);
    if (lane == 0) __hip_atomic_store((gu64*)(ws + WS_FH) + 1024 + ((stile * 4 + r) * 16 + cg), tag, RLX_AGENT);
    POLL_ISSUE(pv, fhp);
    issue_c(13); issue_ab(13);
    WAIT_V(16); BARS(11); issue_c(14); issue_ab(14);
    WAIT_V(16); BARS(12); issue_c(15); issue_ab(15);
    asm volatile("" : "+v"(pv));
    if (!__all(pv == tag)) poll_flags<1>(fhp, tag);
    WAIT_V(16); BARS(13); issue_o(0);
    WAIT_V(16); BARS(14); POLL_ISSUE(pv, fhp + 1024); issue_o(1);
    WAIT_V(16); BARS(15); issue_o(2);
    WAIT_V(16); BARS(16); issue_o(3);
    asm volatile("" : "+v"(pv));
    if (!__all(pv == tag)) poll_flags<1>(fhp + 1024, tag);
#pragma unroll 1
    for (int i = 17; i < 21; ++i) { WAIT_V(16); BARS(i); issue_o(i - 13); }
    gu64* fpp = (gu64*)(ws + WS_FP) + (tile * 4 + sg) * 8 + (lane & 3);
    gu64* fall = (gu64*)(ws + WS_FAB) + ((bid == 0 && r == 0) ? lane * 8 : 0);
    u64 e0, e1, e2, e3;
    WAIT_V(16); BARS(21);
    POLL_ISSUE(e0, fall); POLL_ISSUE(e1, fall + 512); POLL_ISSUE(e2, fall + 1024); POLL_ISSUE(e3, fall + 1536);
    issue_o(8);
    WAIT_V(16); BARS(22); POLL_ISSUE(pv, fpp); issue_o(9);
    WAIT_V(16); BARS(23); issue_o(10);
    const u32 p_src = WS_P + (tile * 4 + sg) * 65536 + r * 4096;
    WAIT_V(16); BARS(24);
    asm volatile("" : "+v"(pv));
    if (!__all(pv == tag || (lane & 3) == sg)) poll_flags<1>(fpp, tag, (lane & 3) == sg);
    DMA4S(ws_r4, lb + blkC(3) + r * 4096, lv, lv, lv, lv, WS_P + (tile * 4 + sg) * 65536 + (sg ^ (2 + (r >> 1))) * 16384 + (r & 1) * 4096);
    if (r < 2) {
      DMA4S(ws_r4, lb + 8 * BLK + r * 4096, lv, lv, lv, lv, WS_P + (tile * 4 + sg) * 65536 + (sg ^ 1) * 16384 + r * 4096);
    } else {
      int xo[4];
#pragma unroll
      for (int ii = 0; ii < 4; ++ii) xo[ii] = (ii * 8 + (lane >> 3)) * 1024 + (((lane & 7) ^ ((ii * 4 + (lane >> 4)) & 7)) << 4) - ii * 1024;
      DMA4(x_rs, lb + 8 * BLK + r * 4096, xo[0], xo[1], xo[2], xo[3], ((sg >> 1) * 64 + (r - 2) * 32) * 1024 + (nt2 * 128 + (sg & 1) * 64) * 2);
    }
    WAIT_V(16); BARS(25);
    WAIT_V(8); BARS(26);
    WAIT_V(0); BARS(27);
    if (bid == 0 && r == 0) {
      asm volatile("" : "+v"(e0), "+v"(e1), "+v"(e2), "+v"(e3));
      if (!__all(e0 == tag && e1 == tag && e2 == tag && e3 == tag)) {
        for (u32 sp = 0; sp < SPIN_CAP; ++sp) {
          bool ok = true;
#pragma unroll
          for (int k = 0; k < 4; ++k) ok &= __hip_atomic_load(fall + k * 512, RLX_AGENT) == tag;
          if (__all(ok)) break;
          __builtin_amdgcn_s_sleep(1);
        }
      }
      if (lane == 0) __hip_atomic_store((gu64*)(ws + WS_EPOCH), tag, RLX_AGENT);
    }
    BAR();
    STAMP(6);
  } else {
    u32 paB[2][2][2];
    preH(accB, 0, bvA, paB);
    if (nh == 3) h_mma(accC, 5 * BLK, 8 * BLK, 2, [&](int) {});
    WAIT_L0();
    BAR();
#pragma unroll
    for (int mt = 0; mt < 2; ++mt) {
      u32x4 v = {paB[mt][0][0], paB[mt][0][1], paB[mt][1][0], paB[mt][1][1]};
      *(LAS u32x4*)(L + 8 * BLK + (r * 2 + mt) * 1024 + lane * 16) = v;
    }
    if (nh == 3) {
      u32 paC[2][2][2];
      preH(accC, 2, bvC, paC);
#pragma unroll
      for (int mt = 0; mt < 2; ++mt) {
        u32x4 v = {paC[mt][0][0], paC[mt][0][1], paC[mt][1][0], paC[mt][1][1]};
        *(LAS u32x4*)(L + 8 * BLK + ((4 + r) * 2 + mt) * 1024 + lane * 16) = v;
      }
    }
    STAMP(4);
    const int th = r >> 1, eh = r & 1;
    const int l31 = lane & 31, hh = lane >> 5;
    f32x16 acc[2][2] = {};
    {
      const u32 a_off = eh * 4096 + ((lane >> 4) & 1) * 1024 + hh * 256 + i16 * 16;
      u32 c_off[4];
#pragma unroll
      for (int k4 = 0; k4 < 4; ++k4) c_off[k4] = (th * 64 + l31) * 128 + (((k4 * 2 + hh) ^ ((l31 >> 1) & 7)) << 4);
      bf16x8 A0[2][2], B0[2][2], A1[2][2], B1[2][2];
      auto rd = [&](bf16x8 (*A)[2], bf16x8 (*B)[2], int st, int kh) {
        LAS char* sa = L + blkAB(st) + a_off;
        LAS char* sc = L + blkC(st);
#pragma unroll
        for (int k2 = 0; k2 < 2; ++k2)
#pragma unroll
          for (int e = 0; e < 2; ++e) {
            A[k2][e] = *(const LAS bf16x8*)(sa + kh * 8192 + e * 2048 + k2 * 512);
            B[k2][e] = *(const LAS bf16x8*)(sc + c_off[kh * 2 + k2] + e * 4096);
          }
      };
      auto mm = [&](bf16x8 (*A)[2], bf16x8 (*B)[2]) {
#pragma unroll
        for (int k2 = 0; k2 < 2; ++k2)
#pragma unroll
          for (int e = 0; e < 2; ++e)
#pragma unroll
            for (int t = 0; t < 2; ++t) acc[e][t] = MFMA32(A[k2][e], B[k2][t], acc[e][t]);
      };
      auto sched = [&]() {
#pragma unroll
        for (int i = 0; i < 4; ++i) {
          __builtin_amdgcn_sched_group_barrier(0x008, 1, 0);
          __builtin_amdgcn_sched_group_barrier(0x100, 2, 0);
        }
        __builtin_amdgcn_sched_group_barrier(0x008, 4, 0);
      };
      __builtin_amdgcn_s_setprio(2);
      WAIT_V(0); WAIT_L0(); BARS(0);
      STAMP(5);
      rd(A0, B0, 0, 0);
#pragma unroll 1
      for (int st = 0; st < 15; ++st) {
        rd(A1, B1, st, 1);
        mm(A0, B0);
        sched();
        WAIT_L0(); BARS(st + 1);
        rd(A0, B0, st + 1, 0);
        mm(A1, B1);
        sched();
        SB0();
      }
      rd(A1, B1, 15, 1);
      mm(A0, B0);
      sched();
      WAIT_L0(); BARS(16);
      mm(A1, B1);
    }
    STAMP(6);
    if (r == sg) {
#pragma unroll
      for (int e = 0; e < 2; ++e)
#pragma unroll
        for (int t = 0; t < 2; ++t)
#pragma unroll
          for (int rg = 0; rg < 4; ++rg) {
            f32x4 v = {acc[e][t][4 * rg], acc[e][t][4 * rg + 1], acc[e][t][4 * rg + 2], acc[e][t][4 * rg + 3]};
            *(LAS f32x4*)(L + MISC + ((e * 2 + t) * 4 + rg) * 1024 + lane * 16) = v;
          }
    }
    const u32 pdst = WS_P + ((tile * 4 + r) * 4 + sg) * 16384 + lane * 16;
    auto pstore = [&](int i) {
      if (r != sg) {
        {
          const int e = i >> 2, t = (i >> 1) & 1, h2 = i & 1;
          u32x4 v = {pack2(acc[e][t][8 * h2], acc[e][t][8 * h2 + 1]), pack2(acc[e][t][8 * h2 + 2], acc[e][t][8 * h2 + 3]),
                     pack2(acc[e][t][8 * h2 + 4], acc[e][t][8 * h2 + 5]), pack2(acc[e][t][8 * h2 + 6], acc[e][t][8 * h2 + 7])};
          __builtin_amdgcn_raw_buffer_store_b128(v, ws_rs, pdst + i * 1024, 0, 16);
        }
        if (false)
#pragma unroll
        for (int u = 2 * i; u < 2 * i + 2; ++u) {
          const int e = u >> 3, t = (u >> 2) & 1, rg = u & 3;
          f32x4 v = {acc[e][t][4 * rg], acc[e][t][4 * rg + 1], acc[e][t][4 * rg + 2], acc[e][t][4 * rg + 3]};
          __builtin_amdgcn_raw_buffer_store_b128(__builtin_bit_cast(u32x4, v), ws_rs, pdst + u * 1024, 0, 16);
        }
      }
    };
    STAMP(7);
    const int dh = r & 1, t2 = r >> 1;
    const int d0 = nt2 * 128 + (sg & 1) * 64 + dh * 32 + 4 * hh;
    f32x16 oc = {};
    u32x2 bv[4];
    {
      const int a = i16 >> 2, p = lane & 3;
      const u32 wb = (8 * hh + a) * 128 + (((dh * 2 + ((lane >> 4) & 1)) ^ (a & 2)) * 32) + p * 8;
      const u32 hb = t2 * 2048 + ((lane >> 4) & 1) * 1024 + hh * 256 + i16 * 16;
      bf16x8 A0[2], B0[2], A1[2], B1[2];
      auto rd = [&](bf16x8* A, bf16x8* B, int j, int kk) {
        LAS char* sw2 = L + blkAB(j) + wb;
        LAS char* sh = L + blkC(j) + hb;
#pragma unroll
        for (int k2 = 0; k2 < 2; ++k2) {
          s16x4 lo = __builtin_amdgcn_ds_read_tr16_b64_v4i16((LAS s16x4*)(sw2 + kk * 4096 + k2 * 2048));
          s16x4 hi = __builtin_amdgcn_ds_read_tr16_b64_v4i16((LAS s16x4*)(sw2 + kk * 4096 + k2 * 2048 + 512));
          A[k2] = __builtin_shufflevector(lo, hi, 0, 1, 2, 3, 4, 5, 6, 7);
          B[k2] = *(const LAS bf16x8*)(sh + kk * 4096 + k2 * 512);
        }
      };
      auto mm = [&](bf16x8* A, bf16x8* B) {
        oc = MFMA32(A[0], B[0], oc);
        oc = MFMA32(A[1], B[1], oc);
      };
      auto sched = [&]() {
        __builtin_amdgcn_sched_group_barrier(0x100, 3, 0);
        __builtin_amdgcn_sched_group_barrier(0x008, 1, 0);
        __builtin_amdgcn_sched_group_barrier(0x100, 3, 0);
        __builtin_amdgcn_sched_group_barrier(0x008, 1, 0);
        SB0();
      };
      rd(A0, B0, 0, 0);
      SB0();
#pragma unroll
      for (int j = 0; j < 2; ++j) {
        rd(A1, B1, j, 1); mm(A0, B0); sched(); pstore(4 * j);
        rd(A0, B0, j, 2); mm(A1, B1); sched(); pstore(4 * j + 1);
        rd(A1, B1, j, 3); mm(A0, B0); sched(); pstore(4 * j + 2);
        WAIT_L0(); BARS(17 + j);
        rd(A0, B0, j + 1, 0); mm(A1, B1); sched(); pstore(4 * j + 3);
      }
#pragma unroll
      for (int rg = 0; rg < 4; ++rg) {
        asm volatile("global_load_dwordx2 %0, %1, off" : "=v"(bv[rg]) : "v"(b2 + d0 + rg * 8) : "memory");
      }
#pragma unroll 1
      for (int j = 2; j < 10; ++j) {
        rd(A1, B1, j, 1); mm(A0, B0); sched();
        rd(A0, B0, j, 2); mm(A1, B1); sched();
        rd(A1, B1, j, 3); mm(A0, B0); sched();
        if (j == 3) {
          WAIT_V(0);
          if (lane == 0 && r != sg) __hip_atomic_store((gu64*)(ws + WS_FP) + (tile * 4 + r) * 8 + sg, tag, RLX_AGENT);
        }
        WAIT_L0(); BARS(17 + j);
        rd(A0, B0, j + 1, 0); mm(A1, B1); sched();
      }
      rd(A1, B1, 10, 1); mm(A0, B0); sched();
      rd(A0, B0, 10, 2); mm(A1, B1); sched();
      mm(A0, B0);
    }
    STAMP(8);
    WAIT_V(0);
#pragma unroll
    for (int rg = 0; rg < 4; ++rg) asm volatile("" : "+v"(bv[rg]));
    float gate[16];
#pragma unroll
    for (int rg = 0; rg < 4; ++rg) {
      const float bb[4] = {lo_f(bv[rg][0]), hi_f(bv[rg][0]), lo_f(bv[rg][1]), hi_f(bv[rg][1])};
#pragma unroll
      for (int j = 0; j < 4; ++j) {
        const float o2 = oc[4 * rg + j] + bb[j];
        gate[4 * rg + j] = o2 * sigm(o2);
      }
    }
#pragma unroll
    for (int j = 0; j < 16; j += 4) asm volatile("" : "+v"(gate[j]), "+v"(gate[j + 1]), "+v"(gate[j + 2]), "+v"(gate[j + 3]));
    BARS(27);
#pragma unroll
    for (int rg = 0; rg < 4; ++rg) {
      const u32 o = ((dh * 2 + t2) * 4 + rg) * 1024 + lane * 16;
      const f32x4 y0 = *(const LAS f32x4*)(L + MISC + o);
      const u32 ob = ((dh * 2 + t2) * 2 + (rg >> 1)) * 1024 + lane * 16 + (rg & 1) * 8;
      const u32x2 q1 = *(const LAS u32x2*)(L + 8 * BLK + ob), q2 = *(const LAS u32x2*)(L + 3 * BLK + ob), q3 = *(const LAS u32x2*)(L + 3 * BLK + 8192 + ob);
      LAS u32x2* xp = (LAS u32x2*)(L + 8 * BLK + 8192 + (t2 * 32 + l31) * 128 + (((dh * 4 + rg) ^ ((l31 >> 1) & 7)) << 4) + hh * 8);
      const u32x2 xv = *xp;
      const float ya[4] = {y0[0] + lo_f(q1[0]), y0[1] + hi_f(q1[0]), y0[2] + lo_f(q1[1]), y0[3] + hi_f(q1[1])};
      const float yb[4] = {lo_f(q2[0]) + lo_f(q3[0]), hi_f(q2[0]) + hi_f(q3[0]), lo_f(q2[1]) + lo_f(q3[1]), hi_f(q2[1]) + hi_f(q3[1])};
      const float xx[4] = {lo_f(xv[0]), hi_f(xv[0]), lo_f(xv[1]), hi_f(xv[1])};
      float res[4];
#pragma unroll
      for (int j = 0; j < 4; ++j) res[j] = xx[j] + (ya[j] + yb[j]) * gate[4 * rg + j];
      u32x2 ov = {pack2(res[0], res[1]), pack2(res[2], res[3])};
      *xp = ov;
    }
    WAIT_L0();
    BAR();
    STAMP(9);
  }
  {
    const int row = tid >> 3, ch = tid & 7;
    const u32x4 v = *(const LAS u32x4*)(L + 8 * BLK + 8192 + row * 128 + ch * 16);
    *(u32x4*)(out + (size_t)(stile * 128 + (sg >> 1) * 64 + row) * 512 + nt2 * 128 + (sg & 1) * 64 + ((ch ^ ((row >> 1) & 7)) << 3)) = v;
  }
  STAMP_FLUSH();
}

extern "C" void kernel_launch(void* const* d_in, const int* in_sizes, int n_in, void* d_out, int out_size, void* d_ws, size_t ws_size,
                              hipStream_t stream) {
  fused_k<<<256, 512, 0, stream>>>((const u16*)d_in[0], (const u16*)d_in[1], (const u16*)d_in[2], (const u16*)d_in[3], (const u16*)d_in[4],
                                   (const u16*)d_in[6], (const u16*)d_in[7], (const u16*)d_in[8], (const u16*)d_in[9], (u16*)d_out,
                                   (char*)d_ws);
}
```

```cpp
#include <hip/hip_runtime.h>
#include <hip/hip_bf16.h>
#include <stdint.h>

typedef unsigned short u16;
typedef unsigned int u32;
typedef unsigned long long u64;
typedef __attribute__((ext_vector_type(8))) short bf16x8;
typedef __attribute__((ext_vector_type(4))) short s16x4;
typedef __attribute__((ext_vector_type(2))) __bf16 bf2;
typedef __attribute__((ext_vector_type(4))) float f32x4;
typedef __attribute__((ext_vector_type(2))) float f32x2;
typedef __attribute__((ext_vector_type(16))) float f32x16;
typedef __attribute__((ext_vector_type(4))) unsigned u32x4;
typedef __attribute__((ext_vector_type(2))) unsigned u32x2;
#define LAS __attribute__((address_space(3)))
#define GAS __attribute__((address_space(1)))
typedef GAS u64 gu64;

#define PBF16 1
constexpr u32 WS_EPOCH = 0;
constexpr u32 WS_FAB = 4096;
constexpr u32 WS_FH = WS_FAB + 16384;
constexpr u32 WS_FP = WS_FH + 16384;
constexpr u32 WS_AB = 1u << 20;
constexpr u32 WS_H = WS_AB + (4u << 20);
constexpr u32 WS_P = WS_H + (6u << 20);
constexpr u32 WS_DBG = WS_P + (16u << 20);
constexpr u32 WS_END = WS_DBG + (2u << 20);

constexpr u32 BLK = 16384;
constexpr u32 MISC = 9 * BLK;
constexpr u32 M_STAT = MISC;
constexpr u32 M_WS = MISC + 1024;
constexpr u32 M_CS = MISC + 2048;
constexpr u32 M_DUMMY = MISC + 4096;

#define RLX_AGENT __ATOMIC_RELAXED, __HIP_MEMORY_SCOPE_AGENT
#define WAIT_V(n) asm volatile("s_waitcnt vmcnt(" #n ")" ::: "memory")
#define WAIT_L0() asm volatile("s_waitcnt lgkmcnt(0)" ::: "memory")
#define BAR() __builtin_amdgcn_s_barrier()
#define SB0() __builtin_amdgcn_sched_barrier(0)
#define RFL(x) ((u32)__builtin_amdgcn_readfirstlane((u32)(x)))
#define BARS(i) BAR()
constexpr u32 SPIN_CAP = 1u << 20;

#define STAMP(i) do {} while (0)
#define STAMPD(i) do {} while (0)
#define STAMP_DECL
#define STAMP_FLUSH() do {} while (0)

__device__ __forceinline__ float rbf(float v) { return (float)(__bf16)v; }
__device__ __forceinline__ u32 pack2(float a, float b) { bf2 v = {(__bf16)a, (__bf16)b}; return __builtin_bit_cast(u32, v); }
__device__ __forceinline__ float lo_f(u32 v) { return __builtin_bit_cast(float, v << 16); }
__device__ __forceinline__ float hi_f(u32 v) { return __builtin_bit_cast(float, v & 0xffff0000u); }
__device__ __forceinline__ float sigm(float z) {
  return __builtin_amdgcn_rcpf(1.0f + __builtin_amdgcn_exp2f(-1.4426950408889634f * z));
}
__device__ __forceinline__ float gelu_t(float u) {
  const float z = u * (-2.3022081983f - 0.1029432398f * u * u);
  return u * __builtin_amdgcn_rcpf(1.0f + __builtin_amdgcn_exp2f(z));
}
__device__ __forceinline__ u32x4 mk_rs(const void* p, u32 bytes) {
  const u64 a = (u64)p;
  u32x4 r = {RFL(a), RFL((u32)(a >> 32) & 0xffffu), bytes, 0x00020000u};
  return r;
}
#define DMA4M(MOD, rs, d, v0, v1, v2, v3, so) do { u32 keep_; \
  asm volatile("s_nop 4\n\ts_mov_b32 %0, m0\n\ts_mov_b32 m0, %1\n\ts_nop 0\n\t" \
               "buffer_load_dwordx4 %2, %6, %7 offen " MOD " lds\n\t" \
               "buffer_load_dwordx4 %3, %6, %7 offen offset:1024 " MOD " lds\n\t" \
               "buffer_load_dwordx4 %4, %6, %7 offen offset:2048 " MOD " lds\n\t" \
               "buffer_load_dwordx4 %5, %6, %7 offen offset:3072 " MOD " lds\n\t" \
               "s_mov_b32 m0, %0" \
               : "=&s"(keep_) : "s"(RFL(d)), "v"(v0), "v"(v1), "v"(v2), "v"(v3), "s"(rs), "s"(RFL(so)) : "memory"); } while (0)
#define DMA4(rs, d, v0, v1, v2, v3, so) DMA4M("", rs, d, v0, v1, v2, v3, so)
#define DMA4S(rs, d, v0, v1, v2, v3, so) DMA4M("sc1", rs, d, v0, v1, v2, v3, so)
#define DMA2(rs, d, v0, v1, so) do { u32 keep_; \
  asm volatile("s_nop 4\n\ts_mov_b32 %0, m0\n\ts_mov_b32 m0, %1\n\ts_nop 0\n\t" \
               "buffer_load_dwordx4 %2, %4, %5 offen lds\n\t" \
               "buffer_load_dwordx4 %3, %4, %5 offen offset:1024 lds\n\t" \
               "s_mov_b32 m0, %0" \
               : "=&s"(keep_) : "s"(RFL(d)), "v"(v0), "v"(v1), "s"(rs), "s"(RFL(so)) : "memory"); } while (0)
#define TOUCH(rs, d, v0, so) do { u32 keep_; \
  asm volatile("s_nop 4\n\ts_mov_b32 %0, m0\n\ts_mov_b32 m0, %1\n\ts_nop 0\n\t" \
               "buffer_load_dword %2, %3, %4 offen lds\n\t" \
               "s_mov_b32 m0, %0" \
               : "=&s"(keep_) : "s"(RFL(d)), "v"(v0), "s"(rs), "s"(RFL(so)) : "memory"); } while (0)
#define TOUCHG(d, p) do { u32 keep_; \
  asm volatile("s_mov_b32 %0, m0\n\ts_mov_b32 m0, %1\n\ts_nop 0\n\t" \
               "global_load_lds_dword %2, off\n\t" \
               "s_mov_b32 m0, %0" \
               : "=&s"(keep_) : "s"(RFL(d)), "v"(p) : "memory"); } while (0)
#define MFMA(a, b, c) __builtin_amdgcn_mfma_f32_16x16x32_bf16(a, b, c, 0, 0, 0)
#define MFMA32(a, b, c) __builtin_amdgcn_mfma_f32_32x32x16_bf16(a, b, c, 0, 0, 0)

template <int NPER>
__device__ __forceinline__ void poll_flags(gu64* f, u64 tag, bool skip = false) {
  for (u32 sp = 0; sp < SPIN_CAP; ++sp) {
    bool ok = true;
#pragma unroll
    for (int k = 0; k < NPER; ++k) {
      u64 v = __hip_atomic_load(f + k * 64, RLX_AGENT);
      ok &= (v == tag);
    }
    if (__all(ok || skip)) break;
    __builtin_amdgcn_s_sleep(1);
  }
  asm volatile("" ::: "memory");
}
#define POLL_ISSUE(val, ptr) asm volatile("global_load_dwordx2 %0, %1, off sc1" : "=v"(val) : "v"(ptr) : "memory")

__global__ __launch_bounds__(512, 2) void fused_k(const u16* __restrict__ x, const u16* __restrict__ Cr, const u16* __restrict__ Ci,
                                                   const u16* __restrict__ Wr, const u16* __restrict__ Wi, const u16* __restrict__ w1,
                                                   const u16* __restrict__ b1, const u16* __restrict__ w2, const u16* __restrict__ b2,
                                                   u16* __restrict__ out, char* __restrict__ ws) {
  __shared__ __attribute__((aligned(1024))) char smem[163840];
  const int tid = threadIdx.x, lane = tid & 63;
  const int wave = __builtin_amdgcn_readfirstlane(tid >> 6);
  const int bid = blockIdx.x, xcd = bid & 7, idx = bid >> 3;
  const int stile = (xcd & 1) * 8 + (idx & 7);
  const int sg = xcd >> 1;
  const int nt2 = idx >> 3;
  const int cg = sg * 4 + nt2;
  const int g = wave >> 2, r = wave & 3;
  const int i16 = lane & 15, q = lane >> 4;
  LAS char* L = (LAS char*)smem;
  const u32 lb = (u32)(size_t)L;
  STAMP_DECL;
  STAMP(0);

  const __amdgpu_buffer_rsrc_t ws_rs = __builtin_amdgcn_make_buffer_rsrc((void*)ws, 0, WS_END, 0x00020000);
  const u32x4 ws_r4 = mk_rs(ws, WS_END);
  const u32x4 c_rs = mk_rs((sg < 2 ? Cr : Ci) + (size_t)stile * 128 * 2048 + (sg & 1) * 1024, 128 * 4096);

  const int nh = cg < 11 ? 3 : 2;
  const int hu0 = cg < 11 ? 3 * cg : 33 + 2 * (cg - 11);
  const int hu2 = cg < 11 ? hu0 + 2 : hu0;
  u32x2 bvA[2], bvC[2];
#pragma unroll
  for (int ft = 0; ft < 2; ++ft) {
    asm volatile("global_load_dwordx2 %0, %1, off" : "=v"(bvA[ft]) : "v"(b1 + (hu0 + g) * 32 + ft * 16 + 4 * q) : "memory");
    asm volatile("global_load_dwordx2 %0, %1, off" : "=v"(bvC[ft]) : "v"(b1 + hu2 * 32 + ft * 16 + 4 * q) : "memory");
  }
  u64 tag;
  POLL_ISSUE(tag, (gu64*)(ws + WS_EPOCH));

  const u32x4 x_rs = mk_rs(x + (size_t)stile * 128 * 512, 131072);
  const u32x4 w_rs = mk_rs((cg < 8 ? Wr : Wi) + (size_t)(cg & 7) * 64 * 512, 65536);
  int xq[4], wq[2];
#pragma unroll
  for (int ii = 0; ii < 4; ++ii) xq[ii] = (wave * 16 + 4 * ii + q) * 1024 + ((i16 ^ (4 * ii + q)) << 4) - ii * 1024;
#pragma unroll
  for (int ii = 0; ii < 2; ++ii) wq[ii] = (wave * 8 + 4 * ii + q) * 1024 + ((i16 ^ ((wave * 8 + 4 * ii + q) & 15)) << 4) - ii * 1024;
  {
#pragma unroll
    for (int kq = 0; kq < 3; ++kq) {
      DMA4(x_rs, lb + kq * 3 * BLK + wave * 4096, xq[0], xq[1], xq[2], xq[3], kq * 256);
      DMA2(w_rs, lb + kq * 3 * BLK + 2 * BLK + wave * 2048, wq[0], wq[1], kq * 256);
      if (kq < 2) BAR();
    }
  }
  STAMP(10);

  const u32x4 w1_rs = mk_rs(w1, 512 * 1376 * 2);
  constexpr int W1ST = 16 * 2752 - 1024;
  const int w1vo = (lane >> 2) * 2752 + (((((lane & 3) >> 1) ^ (lane >> 5)) & 1) << 5) + ((lane & 1) << 4);
  auto issue_h = [&](int unit, u32 d0, u32 d1) {
    DMA4(w1_rs, lb + (g ? d1 : d0) + r * 4096, w1vo, w1vo + W1ST, w1vo + 2 * W1ST, w1vo + 3 * W1ST, wave * 64 * 2752 + unit * 64);
  };

  const bf16x8 ones = {0x3f80, 0x3f80, 0x3f80, 0x3f80, 0x3f80, 0x3f80, 0x3f80, 0x3f80};
  bf16x8 xf[2][16];
  f32x4 accA[2][2] = {};
  f32x4 sS = {}, sQ = {}, wsA[2] = {};
  {
    u32 sw[4];
#pragma unroll
    for (int k3 = 0; k3 < 4; ++k3) sw[k3] = i16 * 256 + (((k3 * 4 + q) ^ i16) << 4);
    const u32 xo0 = r * 8192 + g * 4096, xo1 = r * 8192 + (g ^ 1) * 4096, wo = 2 * BLK + g * 8192;
    bf16x8 wf[2][4][2];
#pragma unroll
    for (int kq = 0; kq < 5; ++kq) {
      if (kq < 4) {
        if (kq == 0) WAIT_V(12); else if (kq == 1) WAIT_V(6); else if (kq == 2) WAIT_V(6); else WAIT_V(4);
        if (kq == 0) { asm volatile("" : "+v"(bvA[0]), "+v"(bvA[1]), "+v"(bvC[0]), "+v"(bvC[1]), "+v"(tag)); tag += 1ull; }
        WAIT_L0();
        BAR();
        if (kq == 0) STAMP(1);
      }
      {
        const u32 qb = (kq % 3) * 3 * BLK;
        const int kp = kq - 1;
#pragma unroll
        for (int k3 = 0; k3 < 4; ++k3) {
          if (kq < 4) {
            xf[0][kq * 4 + k3] = *(const LAS bf16x8*)(L + qb + xo0 + sw[k3]);
            xf[1][kq * 4 + k3] = *(const LAS bf16x8*)(L + qb + xo1 + sw[k3]);
#pragma unroll
            for (int nt = 0; nt < 2; ++nt) wf[kq & 1][k3][nt] = *(const LAS bf16x8*)(L + qb + wo + nt * 4096 + sw[k3]);
          }
          if (kq > 0) {
            const int ks = kp * 4 + k3;
#pragma unroll
            for (int nt = 0; nt < 2; ++nt) {
              accA[nt][0] = MFMA(xf[0][ks], wf[kp & 1][k3][nt], accA[nt][0]);
              accA[nt][1] = MFMA(xf[1][ks], wf[kp & 1][k3][nt], accA[nt][1]);
            }
            sS = MFMA(ones, xf[0][ks], sS);
            sQ = MFMA(xf[0][ks], xf[0][ks], sQ);
            if (kq < 4) {
#pragma unroll
              for (int i = 0; i < 4; ++i) {
                __builtin_amdgcn_sched_group_barrier(0x008, 1, 0);
                __builtin_amdgcn_sched_group_barrier(0x100, 1, 0);
              }
              __builtin_amdgcn_sched_group_barrier(0x008, 2, 0);
            }
          }
          SB0();
          if (kq == 1 && k3 == 0) DMA4(x_rs, lb + wave * 4096, xq[0], xq[1], xq[2], xq[3], 768);
          if (kq == 1 && k3 == 1) DMA2(w_rs, lb + 2 * BLK + wave * 2048, wq[0], wq[1], 768);
          if (kq == 2 && k3 == 0) issue_h(hu0, 3 * BLK, 4 * BLK);
          if (kq == 3 && k3 == 0) issue_h(hu0 + 1, 6 * BLK, 7 * BLK);
          if (kq == 3 && k3 == 1) issue_h(hu2, 5 * BLK, 8 * BLK);
          SB0();
        }
      }
      if (kq > 0) {
        const int kp = kq - 1;
        if (r == kp) {
#pragma unroll
          for (int k3 = 0; k3 < 4; ++k3)
#pragma unroll
            for (int nt = 0; nt < 2; ++nt) wsA[nt] = MFMA(ones, wf[kp & 1][k3][nt], wsA[nt]);
        }
        SB0();
      }
    }
  }
  {
    const int j = i16 & 3;
    float d = j == 0 ? sQ[0] : j == 1 ? sQ[1] : j == 2 ? sQ[2] : sQ[3];
    if (q == (i16 >> 2)) { f32x2 v = {sS[0], d}; *(LAS f32x2*)(L + M_STAT + (r * 32 + g * 16 + i16) * 8) = v; }
    if (q == 0) {
      *(LAS float*)(L + M_WS + ((g * 4 + r) * 32 + i16) * 4) = wsA[0][0];
      *(LAS float*)(L + M_WS + ((g * 4 + r) * 32 + 16 + i16) * 4) = wsA[1][0];
    }
  }
  WAIT_V(4);
  WAIT_L0();
  BAR();
  STAMP(2);

  int cvo[4];
#pragma unroll
  for (int ii = 0; ii < 4; ++ii)
    cvo[ii] = ((r * 4 + ii) * 8 + (lane >> 3)) * 4096 + (((lane & 7) ^ (((ii & 1) * 4 + (lane >> 4)) & 7)) << 4) - ii * 1024;
  const int lv = lane * 16;
  auto blkC = [&](int st) -> u32 { return (u32)(st & 3) * BLK; };
  auto blkAB = [&](int st) -> u32 { return ((0x5764u >> ((st & 3) * 4)) & 15u) * BLK; };
  const int rot = 2 * (stile & 7) + (nt2 & 1) + 8 * (nt2 >> 1);
  auto issue_c = [&](int st) { DMA4(c_rs, lb + blkC(st) + r * 4096, cvo[0], cvo[1], cvo[2], cvo[3], ((st + rot) & 15) * 128); };
  const u32 ab_src = WS_AB + (((sg >> 1) * 4 + nt2) * 64 + (sg & 1) * 32) * 8192 + r * 4096;
  auto issue_ab = [&](int st) { DMA4S(ws_r4, lb + blkAB(st) + r * 4096, lv, lv, lv, lv, ab_src + ((st + rot) & 15) * 16384); };

  const int c4 = (q & 1) * 2 + (q >> 1);
  float mH[2], rH[2];
  float mA[2], rA[2], mrow[2][4], rrow[2][4], wsv[2];
  auto epiA = [&](int ks) {
    if (ks == 0) {
#pragma unroll
      for (int mt = 0; mt < 2; ++mt) {
        const f32x2 sv = *(const LAS f32x2*)(L + M_STAT + (r * 32 + (mt ^ g) * 16 + i16) * 8);
        const float mean = sv[0] * (1.f / 512.f);
        const float var = fmaxf(sv[1] * (1.f / 512.f) - mean * mean, 0.f);
        const float mb = rbf(mean);
        const float den = rbf(__builtin_amdgcn_sqrtf(rbf(rbf(var) + 1e-5f)));
        const float inv = __builtin_amdgcn_rcpf(den);
        const float m2 = rbf((mean - mb) * inv);
        const float den2 = rbf(__builtin_amdgcn_sqrtf(rbf(rbf(var * inv * inv) + 1e-5f)));
        mA[mt] = mb; rA[mt] = cg < 8 ? inv : -inv;
        rH[mt] = inv * __builtin_amdgcn_rcpf(den2); mH[mt] = (mb + m2 * den) * rH[mt];
      }
    } else if (ks == 1) {
#pragma unroll
      for (int mt = 0; mt < 2; ++mt)
#pragma unroll
        for (int j = 0; j < 4; ++j) { mrow[mt][j] = __shfl(mA[mt], 4 * q + j); rrow[mt][j] = __shfl(rA[mt], 4 * q + j); }
#pragma unroll
      for (int nt = 0; nt < 2; ++nt) {
        wsv[nt] = 0.f;
#pragma unroll
        for (int rr = 0; rr < 4; ++rr) wsv[nt] += *(const LAS float*)(L + M_WS + ((g * 4 + rr) * 32 + nt * 16 + i16) * 4);
      }
    } else if (ks == 3 || ks == 5) {
      const int nt = ks == 5;
      const u32 dst = WS_AB + ((((cg >> 3) * 4 + ((cg & 7) >> 1)) * 64 + stile * 4 + r) * 8 + (cg & 1) * 4 + g * 2) * 1024 + ((c4 ^ (g * 2)) * 16 + i16) * 16;
      u32 p[2][2];
#pragma unroll
      for (int mt = 0; mt < 2; ++mt) {
        float v[4];
#pragma unroll
        for (int j = 0; j < 4; ++j) v[j] = (accA[nt][mt][j] - mrow[mt][j] * wsv[nt]) * rrow[mt][j];
        p[mt][0] = pack2(v[0], v[1]); p[mt][1] = pack2(v[2], v[3]);
      }
      auto s0 = __builtin_amdgcn_permlane16_swap(p[0][0], p[1][0], false, false);
      auto s1 = __builtin_amdgcn_permlane16_swap(p[0][1], p[1][1], false, false);
      u32x4 sv = {s0[0], s1[0], s0[1], s1[1]};
      __builtin_amdgcn_raw_buffer_store_b128(sv, ws_rs, dst + nt * 1024, 0, 16);
    }
  };
  epiA(0); epiA(1); epiA(3); epiA(5);
  SB0();
  STAMP(15);
  auto sideB = [&](int ks) {
    if (ks == 6) {
      WAIT_V(0);
      if (lane == 0)
        __hip_atomic_store((gu64*)(ws + WS_FAB) + (((((cg >> 3) * 4 + ((cg & 7) >> 1)) * 16 + stile) * 2 + (cg & 1)) * 8 + wave), tag, RLX_AGENT);
    } else if (ks == 8 || ks == 10 || ks == 12) {
      if (g == 0) issue_c((ks - 8) >> 1);
    }
  };

  auto h_mma = [&](f32x4 (&acc)[2][2], u32 d0, u32 d1, int slot, auto&& side) {
    const int a = i16 >> 2, p = lane & 3;
    u32 tb[2][2];
#pragma unroll
    for (int ft = 0; ft < 2; ++ft) {
      const u32 o = (8 * q + a) * 64 + ((ft ^ (q & 1)) * 32) + p * 8;
      tb[0][ft] = d0 + o; tb[1][ft] = d1 + o;
      acc[ft][0] = (f32x4){0.f, 0.f, 0.f, 0.f}; acc[ft][1] = (f32x4){0.f, 0.f, 0.f, 0.f};
    }
    {
      f32x4 cs[2] = {};
      const u32 cb = (r < 2 ? d0 : d1) + (r & 1) * 8192 - d0;
#pragma unroll
      for (int k = 0; k < 4; ++k)
#pragma unroll
        for (int ft = 0; ft < 2; ++ft) {
          s16x4 lo = __builtin_amdgcn_ds_read_tr16_b64_v4i16((LAS s16x4*)(L + tb[0][ft] + cb + k * 2048));
          s16x4 hi = __builtin_amdgcn_ds_read_tr16_b64_v4i16((LAS s16x4*)(L + tb[0][ft] + cb + k * 2048 + 256));
          cs[ft] = MFMA(__builtin_shufflevector(lo, hi, 0, 1, 2, 3, 4, 5, 6, 7), ones, cs[ft]);
        }
      if (i16 == 0) {
        *(LAS f32x4*)(L + M_CS + ((slot * 4 + r) * 32 + 4 * q) * 4) = cs[0];
        *(LAS f32x4*)(L + M_CS + ((slot * 4 + r) * 32 + 16 + 4 * q) * 4) = cs[1];
      }
    }
    bf16x8 wf[3][2];
    auto ldw = [&](int ks) {
#pragma unroll
      for (int ft = 0; ft < 2; ++ft) {
        s16x4 lo = __builtin_amdgcn_ds_read_tr16_b64_v4i16((LAS s16x4*)(L + tb[ks >> 3][ft] + (ks & 7) * 2048));
        s16x4 hi = __builtin_amdgcn_ds_read_tr16_b64_v4i16((LAS s16x4*)(L + tb[ks >> 3][ft] + (ks & 7) * 2048 + 256));
        wf[ks % 3][ft] = __builtin_shufflevector(lo, hi, 0, 1, 2, 3, 4, 5, 6, 7);
      }
    };
    ldw(0); ldw(1);
    SB0();
#pragma unroll
    for (int ks = 0; ks < 16; ++ks) {
      if (ks + 2 < 16) ldw(ks + 2);
#pragma unroll
      for (int ft = 0; ft < 2; ++ft) {
        acc[ft][0] = MFMA(wf[ks % 3][ft], xf[0][ks], acc[ft][0]);
        acc[ft][1] = MFMA(wf[ks % 3][ft], xf[1][ks], acc[ft][1]);
      }
      side(ks);
      SB0();
    }
  };
  auto preH = [&](f32x4 (&acc)[2][2], int slot, const u32x2* bv, u32 (&pa)[2][2][2]) {
#pragma unroll
    for (int ft = 0; ft < 2; ++ft) {
      f32x4 ecs = *(const LAS f32x4*)(L + M_CS + ((slot * 4 + 0) * 32 + ft * 16 + 4 * q) * 4);
#pragma unroll
      for (int rr = 1; rr < 4; ++rr) ecs += *(const LAS f32x4*)(L + M_CS + ((slot * 4 + rr) * 32 + ft * 16 + 4 * q) * 4);
      const float bj[4] = {lo_f(bv[ft][0]), hi_f(bv[ft][0]), lo_f(bv[ft][1]), hi_f(bv[ft][1])};
#pragma unroll
      for (int mt = 0; mt < 2; ++mt) {
        float v[4];
#pragma unroll
        for (int jj = 0; jj < 4; ++jj) v[jj] = acc[ft][mt][jj] * rH[mt] + (bj[jj] - mH[mt] * ecs[jj]);
        pa[mt][ft][0] = pack2(v[0], v[1]); pa[mt][ft][1] = pack2(v[2], v[3]);
      }
    }
  };
  auto actH = [&](u32 p00, u32 p01, u32 p10, u32 p11, int unit, int trow16) {
    const u32 dst = WS_H + (((stile * 2 + (r >> 1)) * 43 + unit) * 4 + (r & 1) * 2 + trow16) * 1024 + (c4 * 16 + i16) * 16;
    const u32 k00 = pack2(gelu_t(lo_f(p00)), gelu_t(hi_f(p00))), k01 = pack2(gelu_t(lo_f(p01)), gelu_t(hi_f(p01)));
    const u32 k10 = pack2(gelu_t(lo_f(p10)), gelu_t(hi_f(p10))), k11 = pack2(gelu_t(lo_f(p11)), gelu_t(hi_f(p11)));
    auto s0 = __builtin_amdgcn_permlane16_swap(k00, k10, false, false);
    auto s1 = __builtin_amdgcn_permlane16_swap(k01, k11, false, false);
    u32x4 sv = {s0[0], s1[0], s0[1], s1[1]};
    __builtin_amdgcn_raw_buffer_store_b128(sv, ws_rs, dst, 0, 16);
  };
  auto act2 = [&](u32 p) -> u32 { return pack2(gelu_t(lo_f(p)), gelu_t(hi_f(p))); };
  auto actH_fin = [&](u32 k00, u32 k01, u32 p10, u32 p11, int unit, int trow16) {
    const u32 dst = WS_H + (((stile * 2 + (r >> 1)) * 43 + unit) * 4 + (r & 1) * 2 + trow16) * 1024 + (c4 * 16 + i16) * 16;
    auto s0 = __builtin_amdgcn_permlane16_swap(k00, act2(p10), false, false);
    auto s1 = __builtin_amdgcn_permlane16_swap(k01, act2(p11), false, false);
    u32x4 sv = {s0[0], s1[0], s0[1], s1[1]};
    __builtin_amdgcn_raw_buffer_store_b128(sv, ws_rs, dst, 0, 16);
  };
  f32x4 accB[2][2], accC[2][2];
  h_mma(accB, (3 + 3 * g) * BLK, (4 + 3 * g) * BLK, g, sideB);
  STAMP(14);
  WAIT_L0();
  BAR();
  STAMP(3);

  const int tile = stile * 4 + nt2;
  if (g == 1) {
    u32 pa[2][2][2];
    preH(accB, 1, bvA, pa);
    poll_flags<2>((gu64*)(ws + WS_FAB) + (((sg >> 1) * 4 + nt2) * 16 + (sg & 1) * 8) * 16 + lane, tag);
    STAMP(4);
    issue_ab(0); issue_ab(1); issue_ab(2); issue_c(3);
    const u32 h_src = WS_H + (stile * 2 + (sg >> 1)) * 43 * 4096;
    const u32x4 w2_rs = mk_rs(w2, 1376 * 512 * 2);
    int wvo[4];
#pragma unroll
    for (int ii = 0; ii < 4; ++ii) {
      const int c = lane & 7, f = ((lane >> 4) & 1) << 1;
      wvo[ii] = (ii * 8 + (lane >> 3)) * 1024 + (nt2 * 128 + (sg & 1) * 64) * 2 + ((((c >> 1) ^ f) << 5) | ((c & 1) << 4)) - ii * 1024;
    }
    const int orot = stile + nt2;
    auto issue_o = [&](int j) {
      int unit;
      if (j < 4) {
        const int c = ((j + orot) & 3) * 4 + r;
        unit = c < 11 ? 3 * c + 1 : 2 * c + 12;
      } else {
        int jj = j - 4 + (orot & 3) + ((orot >> 2) & 1); jj = jj >= 6 ? jj - 6 : jj; jj = j == 10 ? 6 : jj;
        const int t = min(jj * 4 + r, 26);
        unit = t < 22 ? 3 * (t >> 1) + 2 * (t & 1) : 2 * t - 11;
      }
      DMA4S(ws_r4, lb + blkC(j) + r * 4096, lv, lv, lv, lv, h_src + unit * 4096);
      DMA4(w2_rs, lb + blkAB(j) + r * 4096, wvo[0], wvo[1], wvo[2], wvo[3], unit * 32768);
    };
    STAMP(12);
    BAR();
    STAMP(13);
    issue_ab(3);
    actH(pa[0][0][0], pa[0][0][1], pa[0][1][0], pa[0][1][1], hu0 + 1, 1);
    actH(pa[1][0][0], pa[1][0][1], pa[1][1][0], pa[1][1][1], hu0 + 1, 0);
    WAIT_V(18); BARS(0);
    STAMP(5);
    u32x4 pk_;
    u32 ka, kb;
    auto half_a = [&](int u, int mt) {
      pk_ = *(const LAS u32x4*)(L + 8 * BLK + ((u * 4 + r) * 2 + mt) * 1024 + lane * 16);
      ka = act2(pk_[0]); kb = act2(pk_[1]);
      asm volatile("" : "+v"(ka), "+v"(kb));
    };
    auto half_b = [&](int mt, int unit) { actH_fin(ka, kb, pk_[2], pk_[3], unit, mt); };
    WAIT_V(14); BARS(1); issue_c(4); issue_ab(4);
    half_a(0, 0);
    WAIT_V(8); BARS(2);
    if (lane == 0) __hip_atomic_store((gu64*)(ws + WS_FH) + ((stile * 4 + r) * 16 + cg), tag, RLX_AGENT);
    issue_c(5); issue_ab(5);
    half_b(0, hu0);
    WAIT_V(16); BARS(3); issue_c(6); issue_ab(6);
    half_a(0, 1);
    WAIT_V(16); BARS(4); issue_c(7); issue_ab(7);
    half_b(1, hu0);
    WAIT_V(16); BARS(5); issue_c(8); issue_ab(8);
    if (nh == 3) half_a(1, 0);
    WAIT_V(16); BARS(6); issue_c(9); issue_ab(9);
    if (nh == 3) half_b(0, hu2);
    WAIT_V(16); BARS(7); issue_c(10); issue_ab(10);
    if (nh == 3) half_a(1, 1);
    WAIT_V(16); BARS(8); issue_c(11); issue_ab(11);
    if (nh == 3) half_b(1, hu2);
    WAIT_V(16); BARS(9); issue_c(12); issue_ab(12);
    {
      const int row = (stile & 7) * 172 + min(r * 64 + lane, 171);
      TOUCH(w2_rs, lb + 8 * BLK + r * 256, row * 1024 + (nt2 * 128 + (sg & 1) * 64) * 2, 0);
    }
    gu64* fhp = (gu64*)(ws + WS_FH) + (stile * 4 + 2 * (sg >> 1)) * 16 + (lane & 31);
    u64 pv;
    WAIT_V(8); BARS(10);
    if (lane == 0) __hip_atomic_store((gu64*)(ws + WS_FH) + 1024 + ((stile * 4 + r) * 16 + cg), tag, RLX_AGENT);
    POLL_ISSUE(pv, fhp);
    issue_c(13); issue_ab(13);
    WAIT_V(16); BARS(11); issue_c(14); issue_ab(14);
    WAIT_V(16); BARS(12); issue_c(15); issue_ab(15);
    asm volatile("" : "+v"(pv));
    if (!__all(pv == tag)) poll_flags<1>(fhp, tag);
    WAIT_V(16); BARS(13); issue_o(0);
    WAIT_V(16); BARS(14); POLL_ISSUE(pv, fhp + 1024); issue_o(1);
    WAIT_V(16); BARS(15); issue_o(2);
    WAIT_V(16); BARS(16); issue_o(3);
    asm volatile("" : "+v"(pv));
    if (!__all(pv == tag)) poll_flags<1>(fhp + 1024, tag);
#pragma unroll 1
    for (int i = 17; i < 21; ++i) { WAIT_V(16); BARS(i); issue_o(i - 13); }
    gu64* fpp = (gu64*)(ws + WS_FP) + (tile * 4 + sg) * 8 + (lane & 3);
    gu64* fall = (gu64*)(ws + WS_FAB) + ((bid == 0 && r == 0) ? lane * 8 : 0);
    u64 e0, e1, e2, e3;
    WAIT_V(16); BARS(21);
    POLL_ISSUE(e0, fall); POLL_ISSUE(e1, fall + 512); POLL_ISSUE(e2, fall + 1024); POLL_ISSUE(e3, fall + 1536);
    issue_o(8);
    WAIT_V(16); BARS(22); POLL_ISSUE(pv, fpp); issue_o(9);
    WAIT_V(16); BARS(23); issue_o(10);
    const u32 p_src = WS_P + (tile * 4 + sg) * 65536 + r * 4096;
    WAIT_V(16); BARS(24);
    asm volatile("" : "+v"(pv));
    if (!__all(pv == tag || (lane & 3) == sg)) poll_flags<1>(fpp, tag, (lane & 3) == sg);
    DMA4S(ws_r4, lb + blkC(3) + r * 4096, lv, lv, lv, lv, WS_P + (tile * 4 + sg) * 65536 + (sg ^ (2 + (r >> 1))) * 16384 + (r & 1) * 4096);
    if (r < 2) {
      DMA4S(ws_r4, lb + 8 * BLK + r * 4096, lv, lv, lv, lv, WS_P + (tile * 4 + sg) * 65536 + (sg ^ 1) * 16384 + r * 4096);
    } else {
      int xo[4];
#pragma unroll
      for (int ii = 0; ii < 4; ++ii) xo[ii] = (ii * 8 + (lane >> 3)) * 1024 + (((lane & 7) ^ ((ii * 4 + (lane >> 4)) & 7)) << 4) - ii * 1024;
      DMA4(x_rs, lb + 8 * BLK + r * 4096, xo[0], xo[1], xo[2], xo[3], ((sg >> 1) * 64 + (r - 2) * 32) * 1024 + (nt2 * 128 + (sg & 1) * 64) * 2);
    }
    WAIT_V(16); BARS(25);
    WAIT_V(8); BARS(26);
    WAIT_V(0); BARS(27);
    if (bid == 0 && r == 0) {
      asm volatile("" : "+v"(e0), "+v"(e1), "+v"(e2), "+v"(e3));
      if (!__all(e0 == tag && e1 == tag && e2 == tag && e3 == tag)) {
        for (u32 sp = 0; sp < SPIN_CAP; ++sp) {
          bool ok = true;
#pragma unroll
          for (int k = 0; k < 4; ++k) ok &= __hip_atomic_load(fall + k * 512, RLX_AGENT) == tag;
          if (__all(ok)) break;
          __builtin_amdgcn_s_sleep(1);
        }
      }
      if (lane == 0) __hip_atomic_store((gu64*)(ws + WS_EPOCH), tag, RLX_AGENT);
    }
    BAR();
    STAMP(6);
  } else {
    u32 paB[2][2][2];
    preH(accB, 0, bvA, paB);
    STAMP(11);
    if (nh == 3) h_mma(accC, 5 * BLK, 8 * BLK, 2, [&](int) {});
    WAIT_L0();
    STAMP(12);
    BAR();
    STAMP(13);
#pragma unroll
    for (int mt = 0; mt < 2; ++mt) {
      u32x4 v = {paB[mt][0][0], paB[mt][0][1], paB[mt][1][0], paB[mt][1][1]};
      *(LAS u32x4*)(L + 8 * BLK + (r * 2 + mt) * 1024 + lane * 16) = v;
    }
    if (nh == 3) {
      u32 paC[2][2][2];
      preH(accC, 2, bvC, paC);
#pragma unroll
      for (int mt = 0; mt < 2; ++mt) {
        u32x4 v = {paC[mt][0][0], paC[mt][0][1], paC[mt][1][0], paC[mt][1][1]};
        *(LAS u32x4*)(L + 8 * BLK + ((4 + r) * 2 + mt) * 1024 + lane * 16) = v;
      }
    }
    STAMP(4);
    const int th = r >> 1, eh = r & 1;
    const int l31 = lane & 31, hh = lane >> 5;
    f32x16 acc[2][2] = {};
    {
      const u32 a_off = eh * 4096 + ((lane >> 4) & 1) * 1024 + hh * 256 + i16 * 16;
      u32 c_off[4];
#pragma unroll
      for (int k4 = 0; k4 < 4; ++k4) c_off[k4] = (th * 64 + l31) * 128 + (((k4 * 2 + hh) ^ ((l31 >> 1) & 7)) << 4);
      bf16x8 A0[2][2], B0[2][2], A1[2][2], B1[2][2];
      auto rd = [&](bf16x8 (*A)[2], bf16x8 (*B)[2], int st, int kh) {
        LAS char* sa = L + blkAB(st) + a_off;
        LAS char* sc = L + blkC(st);
#pragma unroll
        for (int k2 = 0; k2 < 2; ++k2)
#pragma unroll
          for (int e = 0; e < 2; ++e) {
            A[k2][e] = *(const LAS bf16x8*)(sa + kh * 8192 + e * 2048 + k2 * 512);
            B[k2][e] = *(const LAS bf16x8*)(sc + c_off[kh * 2 + k2] + e * 4096);
          }
      };
      auto mm = [&](bf16x8 (*A)[2], bf16x8 (*B)[2]) {
#pragma unroll
        for (int k2 = 0; k2 < 2; ++k2)
#pragma unroll
          for (int e = 0; e < 2; ++e)
#pragma unroll
            for (int t = 0; t < 2; ++t) acc[e][t] = MFMA32(A[k2][e], B[k2][t], acc[e][t]);
      };
      auto sched = [&]() {
#pragma unroll
        for (int i = 0; i < 4; ++i) {
          __builtin_amdgcn_sched_group_barrier(0x008, 1, 0);
          __builtin_amdgcn_sched_group_barrier(0x100, 2, 0);
        }
        __builtin_amdgcn_sched_group_barrier(0x008, 4, 0);
      };
      __builtin_amdgcn_s_setprio(2);
      WAIT_V(0); WAIT_L0(); BARS(0);
      STAMP(5);
      rd(A0, B0, 0, 0);
#pragma unroll 1
      for (int st = 0; st < 15; ++st) {
        rd(A1, B1, st, 1);
        mm(A0, B0);
        sched();
        WAIT_L0(); BARS(st + 1);
        rd(A0, B0, st + 1, 0);
        mm(A1, B1);
        sched();
        SB0();
      }
      rd(A1, B1, 15, 1);
      mm(A0, B0);
      sched();
      WAIT_L0(); BARS(16);
      mm(A1, B1);
    }
    STAMP(6);
    if (r == sg) {
#pragma unroll
      for (int e = 0; e < 2; ++e)
#pragma unroll
        for (int t = 0; t < 2; ++t)
#pragma unroll
          for (int rg = 0; rg < 4; ++rg) {
            f32x4 v = {acc[e][t][4 * rg], acc[e][t][4 * rg + 1], acc[e][t][4 * rg + 2], acc[e][t][4 * rg + 3]};
            *(LAS f32x4*)(L + MISC + ((e * 2 + t) * 4 + rg) * 1024 + lane * 16) = v;
          }
    }
    const u32 pdst = WS_P + ((tile * 4 + r) * 4 + sg) * 16384 + lane * 16;
    auto pstore = [&](int i) {
      if (r != sg) {
        {
          const int e = i >> 2, t = (i >> 1) & 1, h2 = i & 1;
          u32x4 v = {pack2(acc[e][t][8 * h2], acc[e][t][8 * h2 + 1]), pack2(acc[e][t][8 * h2 + 2], acc[e][t][8 * h2 + 3]),
                     pack2(acc[e][t][8 * h2 + 4], acc[e][t][8 * h2 + 5]), pack2(acc[e][t][8 * h2 + 6], acc[e][t][8 * h2 + 7])};
          __builtin_amdgcn_raw_buffer_store_b128(v, ws_rs, pdst + i * 1024, 0, 16);
        }
        if (false)
#pragma unroll
        for (int u = 2 * i; u < 2 * i + 2; ++u) {
          const int e = u >> 3, t = (u >> 2) & 1, rg = u & 3;
          f32x4 v = {acc[e][t][4 * rg], acc[e][t][4 * rg + 1], acc[e][t][4 * rg + 2], acc[e][t][4 * rg + 3]};
          __builtin_amdgcn_raw_buffer_store_b128(__builtin_bit_cast(u32x4, v), ws_rs, pdst + u * 1024, 0, 16);
        }
      }
    };
    STAMP(7);
    const int dh = r & 1, t2 = r >> 1;
    const int d0 = nt2 * 128 + (sg & 1) * 64 + dh * 32 + 4 * hh;
    f32x16 oc = {};
    u32x2 bv[4];
    {
      const int a = i16 >> 2, p = lane & 3;
      const u32 wb = (8 * hh + a) * 128 + (((dh * 2 + ((lane >> 4) & 1)) ^ (a & 2)) * 32) + p * 8;
      const u32 hb = t2 * 2048 + ((lane >> 4) & 1) * 1024 + hh * 256 + i16 * 16;
      bf16x8 A0[2], B0[2], A1[2], B1[2];
      auto rd = [&](bf16x8* A, bf16x8* B, int j, int kk) {
        LAS char* sw2 = L + blkAB(j) + wb;
        LAS char* sh = L + blkC(j) + hb;
#pragma unroll
        for (int k2 = 0; k2 < 2; ++k2) {
          s16x4 lo = __builtin_amdgcn_ds_read_tr16_b64_v4i16((LAS s16x4*)(sw2 + kk * 4096 + k2 * 2048));
          s16x4 hi = __builtin_amdgcn_ds_read_tr16_b64_v4i16((LAS s16x4*)(sw2 + kk * 4096 + k2 * 2048 + 512));
          A[k2] = __builtin_shufflevector(lo, hi, 0, 1, 2, 3, 4, 5, 6, 7);
          B[k2] = *(const LAS bf16x8*)(sh + kk * 4096 + k2 * 512);
        }
      };
      auto mm = [&](bf16x8* A, bf16x8* B) {
        oc = MFMA32(A[0], B[0], oc);
        oc = MFMA32(A[1], B[1], oc);
      };
      auto sched = [&]() {
        __builtin_amdgcn_sched_group_barrier(0x100, 3, 0);
        __builtin_amdgcn_sched_group_barrier(0x008, 1, 0);
        __builtin_amdgcn_sched_group_barrier(0x100, 3, 0);
        __builtin_amdgcn_sched_group_barrier(0x008, 1, 0);
        SB0();
      };
      rd(A0, B0, 0, 0);
      SB0();
#pragma unroll
      for (int j = 0; j < 2; ++j) {
        rd(A1, B1, j, 1); mm(A0, B0); sched(); pstore(4 * j);
        rd(A0, B0, j, 2); mm(A1, B1); sched(); pstore(4 * j + 1);
        rd(A1, B1, j, 3); mm(A0, B0); sched(); pstore(4 * j + 2);
        WAIT_L0(); BARS(17 + j);
        rd(A0, B0, j + 1, 0); mm(A1, B1); sched(); pstore(4 * j + 3);
      }
#pragma unroll
      for (int rg = 0; rg < 4; ++rg) {
        asm volatile("global_load_dwordx2 %0, %1, off" : "=v"(bv[rg]) : "v"(b2 + d0 + rg * 8) : "memory");
      }
#pragma unroll 1
      for (int j = 2; j < 10; ++j) {
        rd(A1, B1, j, 1); mm(A0, B0); sched();
        rd(A0, B0, j, 2); mm(A1, B1); sched();
        rd(A1, B1, j, 3); mm(A0, B0); sched();
        if (j == 3) {
          WAIT_V(0);
          if (lane == 0 && r != sg) __hip_atomic_store((gu64*)(ws + WS_FP) + (tile * 4 + r) * 8 + sg, tag, RLX_AGENT);
        }
        WAIT_L0(); BARS(17 + j);
        rd(A0, B0, j + 1, 0); mm(A1, B1); sched();
      }
      rd(A1, B1, 10, 1); mm(A0, B0); sched();
      rd(A0, B0, 10, 2); mm(A1, B1); sched();
      mm(A0, B0);
    }
    STAMP(8);
    WAIT_V(0);
#pragma unroll
    for (int rg = 0; rg < 4; ++rg) asm volatile("" : "+v"(bv[rg]));
    float gate[16];
#pragma unroll
    for (int rg = 0; rg < 4; ++rg) {
      const float bb[4] = {lo_f(bv[rg][0]), hi_f(bv[rg][0]), lo_f(bv[rg][1]), hi_f(bv[rg][1])};
#pragma unroll
      for (int j = 0; j < 4; ++j) {
        const float o2 = oc[4 * rg + j] + bb[j];
        gate[4 * rg + j] = o2 * sigm(o2);
      }
    }
#pragma unroll
    for (int j = 0; j < 16; j += 4) asm volatile("" : "+v"(gate[j]), "+v"(gate[j + 1]), "+v"(gate[j + 2]), "+v"(gate[j + 3]));
    BARS(27);
#pragma unroll
    for (int rg = 0; rg < 4; ++rg) {
      const u32 o = ((dh * 2 + t2) * 4 + rg) * 1024 + lane * 16;
      const f32x4 y0 = *(const LAS f32x4*)(L + MISC + o);
      const u32 ob = ((dh * 2 + t2) * 2 + (rg >> 1)) * 1024 + lane * 16 + (rg & 1) * 8;
      const u32x2 q1 = *(const LAS u32x2*)(L + 8 * BLK + ob), q2 = *(const LAS u32x2*)(L + 3 * BLK + ob), q3 = *(const LAS u32x2*)(L + 3 * BLK + 8192 + ob);
      LAS u32x2* xp = (LAS u32x2*)(L + 8 * BLK + 8192 + (t2 * 32 + l31) * 128 + (((dh * 4 + rg) ^ ((l31 >> 1) & 7)) << 4) + hh * 8);
      const u32x2 xv = *xp;
      const float ya[4] = {y0[0] + lo_f(q1[0]), y0[1] + hi_f(q1[0]), y0[2] + lo_f(q1[1]), y0[3] + hi_f(q1[1])};
      const float yb[4] = {lo_f(q2[0]) + lo_f(q3[0]), hi_f(q2[0]) + hi_f(q3[0]), lo_f(q2[1]) + lo_f(q3[1]), hi_f(q2[1]) + hi_f(q3[1])};
      const float xx[4] = {lo_f(xv[0]), hi_f(xv[0]), lo_f(xv[1]), hi_f(xv[1])};
      float res[4];
#pragma unroll
      for (int j = 0; j < 4; ++j) res[j] = xx[j] + (ya[j] + yb[j]) * gate[4 * rg + j];
      u32x2 ov = {pack2(res[0], res[1]), pack2(res[2], res[3])};
      *xp = ov;
    }
    WAIT_L0();
    BAR();
    STAMP(9);
  }
  {
    const int row = tid >> 3, ch = tid & 7;
    const u32x4 v = *(const LAS u32x4*)(L + 8 * BLK + 8192 + row * 128 + ch * 16);
    *(u32x4*)(out + (size_t)(stile * 128 + (sg >> 1) * 64 + row) * 512 + nt2 * 128 + (sg & 1) * 64 + ((ch ^ ((row >> 1) & 7)) << 3)) = v;
  }
  STAMP_FLUSH();
}

extern "C" void kernel_launch(void* const* d_in, const int* in_sizes, int n_in, void* d_out, int out_size, void* d_ws, size_t ws_size,
                              hipStream_t stream) {
  fused_k<<<256, 512, 0, stream>>>((const u16*)d_in[0], (const u16*)d_in[1], (const u16*)d_in[2], (const u16*)d_in[3], (const u16*)d_in[4],
                                   (const u16*)d_in[6], (const u16*)d_in[7], (const u16*)d_in[8], (const u16*)d_in[9], (u16*)d_out,
                                   (char*)d_ws);
}
```

```cpp
#include <hip/hip_runtime.h>
#include <hip/hip_bf16.h>
#include <stdint.h>

typedef unsigned short u16;
typedef unsigned int u32;
typedef unsigned long long u64;
typedef __attribute__((ext_vector_type(8))) short bf16x8;
typedef __attribute__((ext_vector_type(4))) short s16x4;
typedef __attribute__((ext_vector_type(2))) __bf16 bf2;
typedef __attribute__((ext_vector_type(4))) float f32x4;
typedef __attribute__((ext_vector_type(2))) float f32x2;
typedef __attribute__((ext_vector_type(16))) float f32x16;
typedef __attribute__((ext_vector_type(4))) unsigned u32x4;
typedef __attribute__((ext_vector_type(2))) unsigned u32x2;
#define LAS __attribute__((address_space(3)))
#define GAS __attribute__((address_space(1)))
typedef GAS u64 gu64;

#define PBF16 1
constexpr u32 WS_EPOCH = 0;
constexpr u32 WS_FAB = 4096;
constexpr u32 WS_FH = WS_FAB + 16384;
constexpr u32 WS_FP = WS_FH + 16384;
constexpr u32 WS_AB = 1u << 20;
constexpr u32 WS_H = WS_AB + (4u << 20);
constexpr u32 WS_P = WS_H + (6u << 20);
constexpr u32 WS_DBG = WS_P + (16u << 20);
constexpr u32 WS_END = WS_DBG + (2u << 20);

constexpr u32 BLK = 16384;
constexpr u32 MISC = 9 * BLK;
constexpr u32 M_STAT = MISC;
constexpr u32 M_WS = MISC + 1024;
constexpr u32 M_CS = MISC + 2048;
constexpr u32 M_DUMMY = MISC + 4096;

#define RLX_AGENT __ATOMIC_RELAXED, __HIP_MEMORY_SCOPE_AGENT
#define WAIT_V(n) asm volatile("s_waitcnt vmcnt(" #n ")" ::: "memory")
#define WAIT_L0() asm volatile("s_waitcnt lgkmcnt(0)" ::: "memory")
#define BAR() __builtin_amdgcn_s_barrier()
#define SB0() __builtin_amdgcn_sched_barrier(0)
#define RFL(x) ((u32)__builtin_amdgcn_readfirstlane((u32)(x)))
#define BARS(i) BAR()
constexpr u32 SPIN_CAP = 1u << 20;

#define STAMP(i) do {} while (0)
#define STAMPD(i) do {} while (0)
#define STAMP_DECL
#define STAMP_FLUSH() do {} while (0)

__device__ __forceinline__ float rbf(float v) { return (float)(__bf16)v; }
__device__ __forceinline__ u32 pack2(float a, float b) { bf2 v = {(__bf16)a, (__bf16)b}; return __builtin_bit_cast(u32, v); }
__device__ __forceinline__ float lo_f(u32 v) { return __builtin_bit_cast(float, v << 16); }
__device__ __forceinline__ float hi_f(u32 v) { return __builtin_bit_cast(float, v & 0xffff0000u); }
__device__ __forceinline__ float sigm(float z) {
  return __builtin_amdgcn_rcpf(1.0f + __builtin_amdgcn_exp2f(-1.4426950408889634f * z));
}
__device__ __forceinline__ float gelu_t(float u) {
  const float z = u * (-2.3022081983f - 0.1029432398f * u * u);
  return u * __builtin_amdgcn_rcpf(1.0f + __builtin_amdgcn_exp2f(z));
}
__device__ __forceinline__ u32x4 mk_rs(const void* p, u32 bytes) {
  const u64 a = (u64)p;
  u32x4 r = {RFL(a), RFL((u32)(a >> 32) & 0xffffu), bytes, 0x00020000u};
  return r;
}
#define DMA4M(MOD, rs, d, v0, v1, v2, v3, so) do { u32 keep_; \
  asm volatile("s_nop 4\n\ts_mov_b32 %0, m0\n\ts_mov_b32 m0, %1\n\ts_nop 0\n\t" \
               "buffer_load_dwordx4 %2, %6, %7 offen " MOD " lds\n\t" \
               "buffer_load_dwordx4 %3, %6, %7 offen offset:1024 " MOD " lds\n\t" \
               "buffer_load_dwordx4 %4, %6, %7 offen offset:2048 " MOD " lds\n\t" \
               "buffer_load_dwordx4 %5, %6, %7 offen offset:3072 " MOD " lds\n\t" \
               "s_mov_b32 m0, %0" \
               : "=&s"(keep_) : "s"(RFL(d)), "v"(v0), "v"(v1), "v"(v2), "v"(v3), "s"(rs), "s"(RFL(so)) : "memory"); } while (0)
#define DMA4(rs, d, v0, v1, v2, v3, so) DMA4M("", rs, d, v0, v1, v2, v3, so)
#define DMA4S(rs, d, v0, v1, v2, v3, so) DMA4M("sc1", rs, d, v0, v1, v2, v3, so)
#define DMA2(rs, d, v0, v1, so) do { u32 keep_; \
  asm volatile("s_nop 4\n\ts_mov_b32 %0, m0\n\ts_mov_b32 m0, %1\n\ts_nop 0\n\t" \
               "buffer_load_dwordx4 %2, %4, %5 offen lds\n\t" \
               "buffer_load_dwordx4 %3, %4, %5 offen offset:1024 lds\n\t" \
               "s_mov_b32 m0, %0" \
               : "=&s"(keep_) : "s"(RFL(d)), "v"(v0), "v"(v1), "s"(rs), "s"(RFL(so)) : "memory"); } while (0)
#define TOUCH(rs, d, v0, so) do { u32 keep_; \
  asm volatile("s_nop 4\n\ts_mov_b32 %0, m0\n\ts_mov_b32 m0, %1\n\ts_nop 0\n\t" \
               "buffer_load_dword %2, %3, %4 offen lds\n\t" \
               "s_mov_b32 m0, %0" \
               : "=&s"(keep_) : "s"(RFL(d)), "v"(v0), "s"(rs), "s"(RFL(so)) : "memory"); } while (0)
#define TOUCHG(d, p) do { u32 keep_; \
  asm volatile("s_mov_b32 %0, m0\n\ts_mov_b32 m0, %1\n\ts_nop 0\n\t" \
               "global_load_lds_dword %2, off\n\t" \
               "s_mov_b32 m0, %0" \
               : "=&s"(keep_) : "s"(RFL(d)), "v"(p) : "memory"); } while (0)
#define MFMA(a, b, c) __builtin_amdgcn_mfma_f32_16x16x32_bf16(a, b, c, 0, 0, 0)
#define MFMA32(a, b, c) __builtin_amdgcn_mfma_f32_32x32x16_bf16(a, b, c, 0, 0, 0)

template <int NPER>
__device__ __forceinline__ void poll_flags(gu64* f, u64 tag, bool skip = false) {
  for (u32 sp = 0; sp < SPIN_CAP; ++sp) {
    bool ok = true;
#pragma unroll
    for (int k = 0; k < NPER; ++k) {
      u64 v = __hip_atomic_load(f + k * 64, RLX_AGENT);
      ok &= (v == tag);
    }
    if (__all(ok || skip)) break;
    __builtin_amdgcn_s_sleep(1);
  }
  asm volatile("" ::: "memory");
}
#define POLL_ISSUE(val, ptr) asm volatile("global_load_dwordx2 %0, %1, off sc1" : "=v"(val) : "v"(ptr) : "memory")

__global__ __launch_bounds__(512, 2) void fused_k(const u16* __restrict__ x, const u16* __restrict__ Cr, const u16* __restrict__ Ci,
                                                   const u16* __restrict__ Wr, const u16* __restrict__ Wi, const u16* __restrict__ w1,
                                                   const u16* __restrict__ b1, const u16* __restrict__ w2, const u16* __restrict__ b2,
                                                   u16* __restrict__ out, char* __restrict__ ws) {
  __shared__ __attribute__((aligned(1024))) char smem[163840];
  const int tid = threadIdx.x, lane = tid & 63;
  const int wave = __builtin_amdgcn_readfirstlane(tid >> 6);
  const int bid = blockIdx.x, xcd = bid & 7, idx = bid >> 3;
  const int stile = (xcd & 1) * 8 + (idx & 7);
  const int sg = xcd >> 1;
  const int nt2 = idx >> 3;
  const int cg = sg * 4 + nt2;
  const int g = wave >> 2, r = wave & 3;
  const int i16 = lane & 15, q = lane >> 4;
  LAS char* L = (LAS char*)smem;
  const u32 lb = (u32)(size_t)L;
  STAMP_DECL;
  STAMP(0);

  const __amdgpu_buffer_rsrc_t ws_rs = __builtin_amdgcn_make_buffer_rsrc((void*)ws, 0, WS_END, 0x00020000);
  const u32x4 ws_r4 = mk_rs(ws, WS_END);
  const u32x4 c_rs = mk_rs((sg < 2 ? Cr : Ci) + (size_t)stile * 128 * 2048 + (sg & 1) * 1024, 128 * 4096);

  const int nh = cg < 11 ? 3 : 2;
  const int hu0 = cg < 11 ? 3 * cg : 33 + 2 * (cg - 11);
  const int hu2 = cg < 11 ? hu0 + 2 : hu0;
  u32x2 bvA[2], bvC[2];
#pragma unroll
  for (int ft = 0; ft < 2; ++ft) {
    asm volatile("global_load_dwordx2 %0, %1, off" : "=v"(bvA[ft]) : "v"(b1 + (hu0 + g) * 32 + ft * 16 + 4 * q) : "memory");
    asm volatile("global_load_dwordx2 %0, %1, off" : "=v"(bvC[ft]) : "v"(b1 + hu2 * 32 + ft * 16 + 4 * q) : "memory");
  }
  u64 tag;
  POLL_ISSUE(tag, (gu64*)(ws + WS_EPOCH));

  const u32x4 x_rs = mk_rs(x + (size_t)stile * 128 * 512, 131072);
  constexpr u32 SLOT = 24576;
  const u32x4 d_rs = wave < 4 ? x_rs : mk_rs((cg < 8 ? Wr : Wi) + (size_t)(cg & 7) * 64 * 512, 65536);
  int dv[4];
#pragma unroll
  for (int ii = 0; ii < 4; ++ii) dv[ii] = (ii * 8 + (lane >> 3)) * 1024 + (((lane & 7) ^ ((ii * 4 + (lane >> 4)) & 7)) << 4) - ii * 1024;
  const int rot1 = 0;
  auto issue1 = [&](int t) {
    if (wave < 6) DMA4(d_rs, lb + wave * 4096 + (t & 3) * SLOT, dv[0], dv[1], dv[2], dv[3], (wave & 3) * 32768 + ((t + rot1) & 7) * 128);
  };
  const u32x4 w1_rs = mk_rs(w1, 512 * 1376 * 2);
  constexpr int W1ST = 16 * 2752 - 1024;
  const int w1vo = (lane >> 2) * 2752 + (((((lane & 3) >> 1) ^ (lane >> 5)) & 1) << 5) + ((lane & 1) << 4);
  auto issue_hh = [&](int unit, int half, u32 blk) {
    if (wave >= 6) {
      const int p = half * 4 + (wave - 6) * 2;
      DMA4(w1_rs, lb + blk * BLK + (wave - 6) * 8192, w1vo, w1vo + W1ST, w1vo + 2 * W1ST, w1vo + 3 * W1ST, ((p + rot1) & 7) * 64 * 2752 + unit * 64);
      DMA4(w1_rs, lb + blk * BLK + (wave - 6) * 8192 + 4096, w1vo, w1vo + W1ST, w1vo + 2 * W1ST, w1vo + 3 * W1ST, ((p + 1 + rot1) & 7) * 64 * 2752 + unit * 64);
    }
  };
  issue1(0); BAR(); issue1(1); BAR(); issue1(2); issue1(3);
  issue_hh(hu0, 0, 6); issue_hh(hu0, 1, 7); issue_hh(hu0 + 1, 0, 8);
  STAMP(10);

  const bf16x8 ones = {0x3f80, 0x3f80, 0x3f80, 0x3f80, 0x3f80, 0x3f80, 0x3f80, 0x3f80};
  bf16x8 xf[2][16];
  f32x4 accA[2][2] = {};
  f32x4 sS = {}, sQ = {}, wsA[2] = {};
  {
    u32 xo[2][2], wo[2];
#pragma unroll
    for (int k = 0; k < 2; ++k) {
      const u32 sw = i16 * 128 + (((k * 4 + q) ^ (i16 >> 1)) << 4);
      xo[k][0] = r * 4096 + g * 2048 + sw; xo[k][1] = r * 4096 + (g ^ 1) * 2048 + sw;
      wo[k] = 16384 + g * 4096 + sw;
    }
    bf16x8 Wf[2][2][2];
    auto rd = [&](int t) {
      const u32 sb = (t & 3) * SLOT;
#pragma unroll
      for (int k = 0; k < 2; ++k) {
        xf[0][2 * t + k] = *(const LAS bf16x8*)(L + sb + xo[k][0]); xf[1][2 * t + k] = *(const LAS bf16x8*)(L + sb + xo[k][1]);
        Wf[t & 1][k][0] = *(const LAS bf16x8*)(L + sb + wo[k]); Wf[t & 1][k][1] = *(const LAS bf16x8*)(L + sb + wo[k] + 2048);
      }
    };
    if (wave < 6) WAIT_V(12); else WAIT_V(24);
    asm volatile("" : "+v"(bvA[0]), "+v"(bvA[1]), "+v"(bvC[0]), "+v"(bvC[1]), "+v"(tag));
    tag += 1ull;
    BAR();
    STAMP(1);
    rd(0);
    SB0();
#pragma unroll
    for (int t = 0; t < 8; ++t) {
      if (t < 7) {
        if (wave < 6) { if (t == 0) WAIT_V(8); else if (t < 5) WAIT_V(8); else if (t == 5) WAIT_V(4); else WAIT_V(0); }
        WAIT_L0();
        BAR();
        rd(t + 1);
      }
#pragma unroll
      for (int k = 0; k < 2; ++k) {
        const int ks = 2 * t + k;
#pragma unroll
        for (int nt = 0; nt < 2; ++nt) {
          accA[nt][0] = MFMA(xf[0][ks], Wf[t & 1][k][nt], accA[nt][0]);
          accA[nt][1] = MFMA(xf[1][ks], Wf[t & 1][k][nt], accA[nt][1]);
        }
        sS = MFMA(ones, xf[0][ks], sS);
        sQ = MFMA(xf[0][ks], xf[0][ks], sQ);
      }
      if (t < 7) {
#pragma unroll
        for (int i = 0; i < 8; ++i) {
          __builtin_amdgcn_sched_group_barrier(0x008, 1, 0);
          __builtin_amdgcn_sched_group_barrier(0x100, 1, 0);
        }
        __builtin_amdgcn_sched_group_barrier(0x008, 4, 0);
      }
      SB0();
      if (t < 4) issue1(t + 4);
      if (t == 4) issue_hh(hu0 + 1, 1, 0);
      if (t == 6) { issue_hh(hu2, 0, 2); issue_hh(hu2, 1, 3); }
      SB0();
      if (r == (t >> 1)) {
#pragma unroll
        for (int k = 0; k < 2; ++k) { wsA[0] = MFMA(ones, Wf[t & 1][k][0], wsA[0]); wsA[1] = MFMA(ones, Wf[t & 1][k][1], wsA[1]); }
      }
      SB0();
    }
  }
  {
    const int j = i16 & 3;
    float d = j == 0 ? sQ[0] : j == 1 ? sQ[1] : j == 2 ? sQ[2] : sQ[3];
    if (q == (i16 >> 2)) { f32x2 v = {sS[0], d}; *(LAS f32x2*)(L + M_STAT + (r * 32 + g * 16 + i16) * 8) = v; }
    if (q == 0) {
      *(LAS float*)(L + M_WS + ((g * 4 + r) * 32 + i16) * 4) = wsA[0][0];
      *(LAS float*)(L + M_WS + ((g * 4 + r) * 32 + 16 + i16) * 4) = wsA[1][0];
    }
  }
  if (wave >= 6) WAIT_V(16);
  WAIT_L0();
  BAR();
  STAMP(2);

  int cvo[4];
#pragma unroll
  for (int ii = 0; ii < 4; ++ii)
    cvo[ii] = ((r * 4 + ii) * 8 + (lane >> 3)) * 4096 + (((lane & 7) ^ (((ii & 1) * 4 + (lane >> 4)) & 7)) << 4) - ii * 1024;
  const int lv = lane * 16;
  auto blkC = [&](int st) -> u32 { return ((0x0541u >> ((st & 3) * 4)) & 15u) * BLK; };
  auto blkAB = [&](int st) -> u32 { return ((0x2876u >> ((st & 3) * 4)) & 15u) * BLK; };
  const int rot = 2 * (stile & 7) + (nt2 & 1) + 8 * (nt2 >> 1);
  auto issue_c = [&](int st) { DMA4(c_rs, lb + blkC(st) + r * 4096, cvo[0], cvo[1], cvo[2], cvo[3], ((st + rot) & 15) * 128); };
  const u32 ab_src = WS_AB + (((sg >> 1) * 4 + nt2) * 64 + (sg & 1) * 32) * 8192 + r * 4096;
  auto issue_ab = [&](int st) { DMA4S(ws_r4, lb + blkAB(st) + r * 4096, lv, lv, lv, lv, ab_src + ((st + rot) & 15) * 16384); SB0(); };

  const int c4 = (q & 1) * 2 + (q >> 1);
  float mH[2], rH[2];
  float mA[2], rA[2], mrow[2][4], rrow[2][4], wsv[2];
  auto epiA = [&](int ks) {
    if (ks == 0) {
#pragma unroll
      for (int mt = 0; mt < 2; ++mt) {
        const f32x2 sv = *(const LAS f32x2*)(L + M_STAT + (r * 32 + (mt ^ g) * 16 + i16) * 8);
        const float mean = sv[0] * (1.f / 512.f);
        const float var = fmaxf(sv[1] * (1.f / 512.f) - mean * mean, 0.f);
        const float mb = rbf(mean);
        const float den = rbf(__builtin_amdgcn_sqrtf(rbf(rbf(var) + 1e-5f)));
        const float inv = __builtin_amdgcn_rcpf(den);
        const float m2 = rbf((mean - mb) * inv);
        const float den2 = rbf(__builtin_amdgcn_sqrtf(rbf(rbf(var * inv * inv) + 1e-5f)));
        mA[mt] = mb; rA[mt] = cg < 8 ? inv : -inv;
        rH[mt] = inv * __builtin_amdgcn_rcpf(den2); mH[mt] = (mb + m2 * den) * rH[mt];
      }
    } else if (ks == 1) {
#pragma unroll
      for (int mt = 0; mt < 2; ++mt)
#pragma unroll
        for (int j = 0; j < 4; ++j) { mrow[mt][j] = __shfl(mA[mt], 4 * q + j); rrow[mt][j] = __shfl(rA[mt], 4 * q + j); }
#pragma unroll
      for (int nt = 0; nt < 2; ++nt) {
        wsv[nt] = 0.f;
#pragma unroll
        for (int rr = 0; rr < 4; ++rr) wsv[nt] += *(const LAS float*)(L + M_WS + ((g * 4 + rr) * 32 + nt * 16 + i16) * 4);
      }
    } else if (ks == 3 || ks == 5) {
      const int nt = ks == 5;
      const u32 dst = WS_AB + ((((cg >> 3) * 4 + ((cg & 7) >> 1)) * 64 + stile * 4 + r) * 8 + (cg & 1) * 4 + g * 2) * 1024 + ((c4 ^ (g * 2)) * 16 + i16) * 16;
      u32 p[2][2];
#pragma unroll
      for (int mt = 0; mt < 2; ++mt) {
        float v[4];
#pragma unroll
        for (int j = 0; j < 4; ++j) v[j] = (accA[nt][mt][j] - mrow[mt][j] * wsv[nt]) * rrow[mt][j];
        p[mt][0] = pack2(v[0], v[1]); p[mt][1] = pack2(v[2], v[3]);
      }
      auto s0 = __builtin_amdgcn_permlane16_swap(p[0][0], p[1][0], false, false);
      auto s1 = __builtin_amdgcn_permlane16_swap(p[0][1], p[1][1], false, false);
      u32x4 sv = {s0[0], s1[0], s0[1], s1[1]};
      __builtin_amdgcn_raw_buffer_store_b128(sv, ws_rs, dst + nt * 1024, 0, 16);
    }
  };
  epiA(0); epiA(1); epiA(3); epiA(5);
  SB0();
  STAMP(15);
  auto sideB = [&](int ks) {
    if (ks == 6) {
      WAIT_V(0);
      if (lane == 0)
        __hip_atomic_store((gu64*)(ws + WS_FAB) + (((((cg >> 3) * 4 + ((cg & 7) >> 1)) * 16 + stile) * 2 + (cg & 1)) * 8 + wave), tag, RLX_AGENT);
    } else if (ks == 8 || ks == 10 || ks == 12) {
      if (g == 0) issue_c((ks - 8) >> 1);
    }
  };

  auto h_mma = [&](f32x4 (&acc)[2][2], u32 d0, u32 d1, int slot, auto&& side) {
    const int a = i16 >> 2, p = lane & 3;
    u32 tb[2][2];
#pragma unroll
    for (int ft = 0; ft < 2; ++ft) {
      const u32 o = (8 * q + a) * 64 + ((ft ^ (q & 1)) * 32) + p * 8;
      tb[0][ft] = d0 + o; tb[1][ft] = d1 + o;
      acc[ft][0] = (f32x4){0.f, 0.f, 0.f, 0.f}; acc[ft][1] = (f32x4){0.f, 0.f, 0.f, 0.f};
    }
    {
      f32x4 cs[2] = {};
      const u32 cb = (r < 2 ? d0 : d1) + (r & 1) * 8192 - d0;
#pragma unroll
      for (int k = 0; k < 4; ++k)
#pragma unroll
        for (int ft = 0; ft < 2; ++ft) {
          s16x4 lo = __builtin_amdgcn_ds_read_tr16_b64_v4i16((LAS s16x4*)(L + tb[0][ft] + cb + k * 2048));
          s16x4 hi = __builtin_amdgcn_ds_read_tr16_b64_v4i16((LAS s16x4*)(L + tb[0][ft] + cb + k * 2048 + 256));
          cs[ft] = MFMA(__builtin_shufflevector(lo, hi, 0, 1, 2, 3, 4, 5, 6, 7), ones, cs[ft]);
        }
      if (i16 == 0) {
        *(LAS f32x4*)(L + M_CS + ((slot * 4 + r) * 32 + 4 * q) * 4) = cs[0];
        *(LAS f32x4*)(L + M_CS + ((slot * 4 + r) * 32 + 16 + 4 * q) * 4) = cs[1];
      }
    }
    bf16x8 wf[3][2];
    auto ldw = [&](int ks) {
#pragma unroll
      for (int ft = 0; ft < 2; ++ft) {
        s16x4 lo = __builtin_amdgcn_ds_read_tr16_b64_v4i16((LAS s16x4*)(L + tb[ks >> 3][ft] + (ks & 7) * 2048));
        s16x4 hi = __builtin_amdgcn_ds_read_tr16_b64_v4i16((LAS s16x4*)(L + tb[ks >> 3][ft] + (ks & 7) * 2048 + 256));
        wf[ks % 3][ft] = __builtin_shufflevector(lo, hi, 0, 1, 2, 3, 4, 5, 6, 7);
      }
    };
    ldw(0); ldw(1);
    SB0();
#pragma unroll
    for (int ks = 0; ks < 16; ++ks) {
      if (ks + 2 < 16) ldw(ks + 2);
#pragma unroll
      for (int ft = 0; ft < 2; ++ft) {
        acc[ft][0] = MFMA(wf[ks % 3][ft], xf[0][ks], acc[ft][0]);
        acc[ft][1] = MFMA(wf[ks % 3][ft], xf[1][ks], acc[ft][1]);
      }
      side(ks);
      SB0();
    }
  };
  auto preH = [&](f32x4 (&acc)[2][2], int slot, const u32x2* bv, u32 (&pa)[2][2][2]) {
#pragma unroll
    for (int ft = 0; ft < 2; ++ft) {
      f32x4 ecs = *(const LAS f32x4*)(L + M_CS + ((slot * 4 + 0) * 32 + ft * 16 + 4 * q) * 4);
#pragma unroll
      for (int rr = 1; rr < 4; ++rr) ecs += *(const LAS f32x4*)(L + M_CS + ((slot * 4 + rr) * 32 + ft * 16 + 4 * q) * 4);
      const float bj[4] = {lo_f(bv[ft][0]), hi_f(bv[ft][0]), lo_f(bv[ft][1]), hi_f(bv[ft][1])};
#pragma unroll
      for (int mt = 0; mt < 2; ++mt) {
        float v[4];
#pragma unroll
        for (int jj = 0; jj < 4; ++jj) v[jj] = acc[ft][mt][jj] * rH[mt] + (bj[jj] - mH[mt] * ecs[jj]);
        pa[mt][ft][0] = pack2(v[0], v[1]); pa[mt][ft][1] = pack2(v[2], v[3]);
      }
    }
  };
  auto actH = [&](u32 p00, u32 p01, u32 p10, u32 p11, int unit, int trow16) {
    const u32 dst = WS_H + (((stile * 2 + (r >> 1)) * 43 + unit) * 4 + (r & 1) * 2 + trow16) * 1024 + (c4 * 16 + i16) * 16;
    const u32 k00 = pack2(gelu_t(lo_f(p00)), gelu_t(hi_f(p00))), k01 = pack2(gelu_t(lo_f(p01)), gelu_t(hi_f(p01)));
    const u32 k10 = pack2(gelu_t(lo_f(p10)), gelu_t(hi_f(p10))), k11 = pack2(gelu_t(lo_f(p11)), gelu_t(hi_f(p11)));
    auto s0 = __builtin_amdgcn_permlane16_swap(k00, k10, false, false);
    auto s1 = __builtin_amdgcn_permlane16_swap(k01, k11, false, false);
    u32x4 sv = {s0[0], s1[0], s0[1], s1[1]};
    __builtin_amdgcn_raw_buffer_store_b128(sv, ws_rs, dst, 0, 16);
  };
  auto act2 = [&](u32 p) -> u32 { return pack2(gelu_t(lo_f(p)), gelu_t(hi_f(p))); };
  auto actH_fin = [&](u32 k00, u32 k01, u32 p10, u32 p11, int unit, int trow16) {
    const u32 dst = WS_H + (((stile * 2 + (r >> 1)) * 43 + unit) * 4 + (r & 1) * 2 + trow16) * 1024 + (c4 * 16 + i16) * 16;
    auto s0 = __builtin_amdgcn_permlane16_swap(k00, act2(p10), false, false);
    auto s1 = __builtin_amdgcn_permlane16_swap(k01, act2(p11), false, false);
    u32x4 sv = {s0[0], s1[0], s0[1], s1[1]};
    __builtin_amdgcn_raw_buffer_store_b128(sv, ws_rs, dst, 0, 16);
  };
  f32x4 accB[2][2], accC[2][2];
  h_mma(accB, (6 + 2 * g) * BLK, (g ? 0 : 7) * BLK, g, sideB);
  STAMP(14);
  WAIT_L0();
  BAR();
  STAMP(3);

  const int tile = stile * 4 + nt2;
  if (g == 1) {
    u32 pa[2][2][2];
    preH(accB, 1, bvA, pa);
    poll_flags<2>((gu64*)(ws + WS_FAB) + (((sg >> 1) * 4 + nt2) * 16 + (sg & 1) * 8) * 16 + lane, tag);
    STAMP(4);
    issue_ab(0); issue_ab(1); issue_ab(2); issue_c(3);
    const u32 h_src = WS_H + (stile * 2 + (sg >> 1)) * 43 * 4096;
    const u32x4 w2_rs = mk_rs(w2, 1376 * 512 * 2);
    int wvo[4];
#pragma unroll
    for (int ii = 0; ii < 4; ++ii) {
      const int c = lane & 7, f = ((lane >> 4) & 1) << 1;
      wvo[ii] = (ii * 8 + (lane >> 3)) * 1024 + (nt2 * 128 + (sg & 1) * 64) * 2 + ((((c >> 1) ^ f) << 5) | ((c & 1) << 4)) - ii * 1024;
    }
    const int orot = stile + nt2;
    auto issue_o = [&](int j) {
      int unit;
      if (j < 4) {
        const int c = ((j + orot) & 3) * 4 + r;
        unit = c < 11 ? 3 * c + 1 : 2 * c + 12;
      } else {
        int jj = j - 4 + (orot & 3) + ((orot >> 2) & 1); jj = jj >= 6 ? jj - 6 : jj; jj = j == 10 ? 6 : jj;
        const int t = min(jj * 4 + r, 26);
        unit = t < 22 ? 3 * (t >> 1) + 2 * (t & 1) : 2 * t - 11;
      }
      DMA4S(ws_r4, lb + blkC(j) + r * 4096, lv, lv, lv, lv, h_src + unit * 4096);
      DMA4(w2_rs, lb + blkAB(j) + r * 4096, wvo[0], wvo[1], wvo[2], wvo[3], unit * 32768);
      SB0();
    };
    STAMP(12);
    BAR();
    STAMP(13);
    issue_ab(3);
    actH(pa[0][0][0], pa[0][0][1], pa[0][1][0], pa[0][1][1], hu0 + 1, 1);
    actH(pa[1][0][0], pa[1][0][1], pa[1][1][0], pa[1][1][1], hu0 + 1, 0);
    SB0(); WAIT_V(18); BARS(0);
    STAMP(5);
    u32x4 pk_;
    u32 ka, kb;
    auto half_a = [&](int u, int mt) {
      pk_ = *(const LAS u32x4*)(L + 3 * BLK + ((u * 4 + r) * 2 + mt) * 1024 + lane * 16);
      ka = act2(pk_[0]); kb = act2(pk_[1]);
      asm volatile("" : "+v"(ka), "+v"(kb));
    };
    auto half_b = [&](int mt, int unit) { actH_fin(ka, kb, pk_[2], pk_[3], unit, mt); };
    SB0(); WAIT_V(14); BARS(1); issue_c(4); issue_ab(4);
    half_a(0, 0);
    SB0(); WAIT_V(8); BARS(2);
    if (lane == 0) __hip_atomic_store((gu64*)(ws + WS_FH) + ((stile * 4 + r) * 16 + cg), tag, RLX_AGENT);
    issue_c(5); issue_ab(5);
    half_b(0, hu0);
    SB0(); WAIT_V(16); BARS(3); issue_c(6); issue_ab(6);
    half_a(0, 1);
    SB0(); WAIT_V(16); BARS(4); issue_c(7); issue_ab(7);
    half_b(1, hu0);
    SB0(); WAIT_V(16); BARS(5); issue_c(8); issue_ab(8);
    if (nh == 3) half_a(1, 0);
    SB0(); WAIT_V(16); BARS(6); issue_c(9); issue_ab(9);
    if (nh == 3) half_b(0, hu2);
    SB0(); WAIT_V(16); BARS(7); issue_c(10); issue_ab(10);
    if (nh == 3) half_a(1, 1);
    SB0(); WAIT_V(16); BARS(8); issue_c(11); issue_ab(11);
    if (nh == 3) half_b(1, hu2);
    SB0(); WAIT_V(16); BARS(9); issue_c(12); issue_ab(12);
    {
      const int row = (stile & 7) * 172 + min(r * 64 + lane, 171);
      TOUCH(w2_rs, lb + 3 * BLK + r * 256, row * 1024 + (nt2 * 128 + (sg & 1) * 64) * 2, 0);
    }
    gu64* fhp = (gu64*)(ws + WS_FH) + (stile * 4 + 2 * (sg >> 1)) * 16 + (lane & 31);
    u64 pv;
    SB0(); WAIT_V(8); BARS(10);
    if (lane == 0) __hip_atomic_store((gu64*)(ws + WS_FH) + 1024 + ((stile * 4 + r) * 16 + cg), tag, RLX_AGENT);
    POLL_ISSUE(pv, fhp);
    issue_c(13); issue_ab(13);
    SB0(); WAIT_V(16); BARS(11); issue_c(14); issue_ab(14);
    SB0(); WAIT_V(16); BARS(12); issue_c(15); issue_ab(15);
    asm volatile("" : "+v"(pv));
    if (!__all(pv == tag)) poll_flags<1>(fhp, tag);
    SB0(); WAIT_V(16); BARS(13); issue_o(0);
    SB0(); WAIT_V(16); BARS(14); POLL_ISSUE(pv, fhp + 1024); issue_o(1);
    SB0(); WAIT_V(16); BARS(15); issue_o(2);
    SB0(); WAIT_V(16); BARS(16); issue_o(3);
    asm volatile("" : "+v"(pv));
    if (!__all(pv == tag)) poll_flags<1>(fhp + 1024, tag);
#pragma unroll 1
    for (int i = 17; i < 21; ++i) { WAIT_V(16); BARS(i); issue_o(i - 13); }
    gu64* fpp = (gu64*)(ws + WS_FP) + (tile * 4 + sg) * 8 + (lane & 3);
    gu64* fall = (gu64*)(ws + WS_FAB) + ((bid == 0 && r == 0) ? lane * 8 : 0);
    u64 e0, e1, e2, e3;
    SB0(); WAIT_V(16); BARS(21);
    POLL_ISSUE(e0, fall); POLL_ISSUE(e1, fall + 512); POLL_ISSUE(e2, fall + 1024); POLL_ISSUE(e3, fall + 1536);
    issue_o(8);
    SB0(); WAIT_V(16); BARS(22); POLL_ISSUE(pv, fpp); issue_o(9);
    SB0(); WAIT_V(16); BARS(23); issue_o(10);
    const u32 p_src = WS_P + (tile * 4 + sg) * 65536 + r * 4096;
    SB0(); WAIT_V(16); BARS(24);
    asm volatile("" : "+v"(pv));
    if (!__all(pv == tag || (lane & 3) == sg)) poll_flags<1>(fpp, tag, (lane & 3) == sg);
    DMA4S(ws_r4, lb + blkC(3) + r * 4096, lv, lv, lv, lv, WS_P + (tile * 4 + sg) * 65536 + (sg ^ (2 + (r >> 1))) * 16384 + (r & 1) * 4096);
    if (r < 2) {
      DMA4S(ws_r4, lb + 3 * BLK + r * 4096, lv, lv, lv, lv, WS_P + (tile * 4 + sg) * 65536 + (sg ^ 1) * 16384 + r * 4096);
    } else {
      int xo[4];
#pragma unroll
      for (int ii = 0; ii < 4; ++ii) xo[ii] = (ii * 8 + (lane >> 3)) * 1024 + (((lane & 7) ^ ((ii * 4 + (lane >> 4)) & 7)) << 4) - ii * 1024;
      DMA4(x_rs, lb + 3 * BLK + r * 4096, xo[0], xo[1], xo[2], xo[3], ((sg >> 1) * 64 + (r - 2) * 32) * 1024 + (nt2 * 128 + (sg & 1) * 64) * 2);
    }
    SB0(); WAIT_V(16); BARS(25);
    SB0(); WAIT_V(8); BARS(26);
    SB0(); WAIT_V(0); BARS(27);
    if (bid == 0 && r == 0) {
      asm volatile("" : "+v"(e0), "+v"(e1), "+v"(e2), "+v"(e3));
      if (!__all(e0 == tag && e1 == tag && e2 == tag && e3 == tag)) {
        for (u32 sp = 0; sp < SPIN_CAP; ++sp) {
          bool ok = true;
#pragma unroll
          for (int k = 0; k < 4; ++k) ok &= __hip_atomic_load(fall + k * 512, RLX_AGENT) == tag;
          if (__all(ok)) break;
          __builtin_amdgcn_s_sleep(1);
        }
      }
      if (lane == 0) __hip_atomic_store((gu64*)(ws + WS_EPOCH), tag, RLX_AGENT);
    }
    BAR();
    STAMP(6);
  } else {
    u32 paB[2][2][2];
    preH(accB, 0, bvA, paB);
    STAMP(11);
    if (nh == 3) h_mma(accC, 2 * BLK, 3 * BLK, 2, [&](int) {});
    WAIT_L0();
    STAMP(12);
    BAR();
    STAMP(13);
#pragma unroll
    for (int mt = 0; mt < 2; ++mt) {
      u32x4 v = {paB[mt][0][0], paB[mt][0][1], paB[mt][1][0], paB[mt][1][1]};
      *(LAS u32x4*)(L + 3 * BLK + (r * 2 + mt) * 1024 + lane * 16) = v;
    }
    if (nh == 3) {
      u32 paC[2][2][2];
      preH(accC, 2, bvC, paC);
#pragma unroll
      for (int mt = 0; mt < 2; ++mt) {
        u32x4 v = {paC[mt][0][0], paC[mt][0][1], paC[mt][1][0], paC[mt][1][1]};
        *(LAS u32x4*)(L + 3 * BLK + ((4 + r) * 2 + mt) * 1024 + lane * 16) = v;
      }
    }
    STAMP(4);
    const int th = r >> 1, eh = r & 1;
    const int l31 = lane & 31, hh = lane >> 5;
    f32x16 acc[2][2] = {};
    {
      const u32 a_off = eh * 4096 + ((lane >> 4) & 1) * 1024 + hh * 256 + i16 * 16;
      u32 c_off[4];
#pragma unroll
      for (int k4 = 0; k4 < 4; ++k4) c_off[k4] = (th * 64 + l31) * 128 + (((k4 * 2 + hh) ^ ((l31 >> 1) & 7)) << 4);
      bf16x8 A0[2][2], B0[2][2], A1[2][2], B1[2][2];
      auto rd = [&](bf16x8 (*A)[2], bf16x8 (*B)[2], int st, int kh) {
        LAS char* sa = L + blkAB(st) + a_off;
        LAS char* sc = L + blkC(st);
#pragma unroll
        for (int k2 = 0; k2 < 2; ++k2)
#pragma unroll
          for (int e = 0; e < 2; ++e) {
            A[k2][e] = *(const LAS bf16x8*)(sa + kh * 8192 + e * 2048 + k2 * 512);
            B[k2][e] = *(const LAS bf16x8*)(sc + c_off[kh * 2 + k2] + e * 4096);
          }
      };
      auto mm = [&](bf16x8 (*A)[2], bf16x8 (*B)[2]) {
#pragma unroll
        for (int k2 = 0; k2 < 2; ++k2)
#pragma unroll
          for (int e = 0; e < 2; ++e)
#pragma unroll
            for (int t = 0; t < 2; ++t) acc[e][t] = MFMA32(A[k2][e], B[k2][t], acc[e][t]);
      };
      auto sched = [&]() {
#pragma unroll
        for (int i = 0; i < 4; ++i) {
          __builtin_amdgcn_sched_group_barrier(0x008, 1, 0);
          __builtin_amdgcn_sched_group_barrier(0x100, 2, 0);
        }
        __builtin_amdgcn_sched_group_barrier(0x008, 4, 0);
      };
      __builtin_amdgcn_s_setprio(2);
      WAIT_V(0); WAIT_L0(); BARS(0);
      STAMP(5);
      rd(A0, B0, 0, 0);
#pragma unroll 1
      for (int st = 0; st < 15; ++st) {
        rd(A1, B1, st, 1);
        mm(A0, B0);
        sched();
        WAIT_L0(); BARS(st + 1);
        rd(A0, B0, st + 1, 0);
        mm(A1, B1);
        sched();
        SB0();
      }
      rd(A1, B1, 15, 1);
      mm(A0, B0);
      sched();
      WAIT_L0(); BARS(16);
      mm(A1, B1);
    }
    STAMP(6);
    if (r == sg) {
#pragma unroll
      for (int e = 0; e < 2; ++e)
#pragma unroll
        for (int t = 0; t < 2; ++t)
#pragma unroll
          for (int rg = 0; rg < 4; ++rg) {
            f32x4 v = {acc[e][t][4 * rg], acc[e][t][4 * rg + 1], acc[e][t][4 * rg + 2], acc[e][t][4 * rg + 3]};
            *(LAS f32x4*)(L + MISC + ((e * 2 + t) * 4 + rg) * 1024 + lane * 16) = v;
          }
    }
    const u32 pdst = WS_P + ((tile * 4 + r) * 4 + sg) * 16384 + lane * 16;
    auto pstore = [&](int i) {
      if (r != sg) {
        {
          const int e = i >> 2, t = (i >> 1) & 1, h2 = i & 1;
          u32x4 v = {pack2(acc[e][t][8 * h2], acc[e][t][8 * h2 + 1]), pack2(acc[e][t][8 * h2 + 2], acc[e][t][8 * h2 + 3]),
                     pack2(acc[e][t][8 * h2 + 4], acc[e][t][8 * h2 + 5]), pack2(acc[e][t][8 * h2 + 6], acc[e][t][8 * h2 + 7])};
          __builtin_amdgcn_raw_buffer_store_b128(v, ws_rs, pdst + i * 1024, 0, 16);
        }
        if (false)
#pragma unroll
        for (int u = 2 * i; u < 2 * i + 2; ++u) {
          const int e = u >> 3, t = (u >> 2) & 1, rg = u & 3;
          f32x4 v = {acc[e][t][4 * rg], acc[e][t][4 * rg + 1], acc[e][t][4 * rg + 2], acc[e][t][4 * rg + 3]};
          __builtin_amdgcn_raw_buffer_store_b128(__builtin_bit_cast(u32x4, v), ws_rs, pdst + u * 1024, 0, 16);
        }
      }
    };
    STAMP(7);
    const int dh = r & 1, t2 = r >> 1;
    const int d0 = nt2 * 128 + (sg & 1) * 64 + dh * 32 + 4 * hh;
    f32x16 oc = {};
    u32x2 bv[4];
    {
      const int a = i16 >> 2, p = lane & 3;
      const u32 wb = (8 * hh + a) * 128 + (((dh * 2 + ((lane >> 4) & 1)) ^ (a & 2)) * 32) + p * 8;
      const u32 hb = t2 * 2048 + ((lane >> 4) & 1) * 1024 + hh * 256 + i16 * 16;
      bf16x8 A0[2], B0[2], A1[2], B1[2];
      auto rd = [&](bf16x8* A, bf16x8* B, int j, int kk) {
        LAS char* sw2 = L + blkAB(j) + wb;
        LAS char* sh = L + blkC(j) + hb;
#pragma unroll
        for (int k2 = 0; k2 < 2; ++k2) {
          s16x4 lo = __builtin_amdgcn_ds_read_tr16_b64_v4i16((LAS s16x4*)(sw2 + kk * 4096 + k2 * 2048));
          s16x4 hi = __builtin_amdgcn_ds_read_tr16_b64_v4i16((LAS s16x4*)(sw2 + kk * 4096 + k2 * 2048 + 512));
          A[k2] = __builtin_shufflevector(lo, hi, 0, 1, 2, 3, 4, 5, 6, 7);
          B[k2] = *(const LAS bf16x8*)(sh + kk * 4096 + k2 * 512);
        }
      };
      auto mm = [&](bf16x8* A, bf16x8* B) {
        oc = MFMA32(A[0], B[0], oc);
        oc = MFMA32(A[1], B[1], oc);
      };
      auto sched = [&]() {
        __builtin_amdgcn_sched_group_barrier(0x100, 3, 0);
        __builtin_amdgcn_sched_group_barrier(0x008, 1, 0);
        __builtin_amdgcn_sched_group_barrier(0x100, 3, 0);
        __builtin_amdgcn_sched_group_barrier(0x008, 1, 0);
        SB0();
      };
      rd(A0, B0, 0, 0);
      SB0();
#pragma unroll
      for (int j = 0; j < 2; ++j) {
        rd(A1, B1, j, 1); mm(A0, B0); sched(); pstore(4 * j);
        rd(A0, B0, j, 2); mm(A1, B1); sched(); pstore(4 * j + 1);
        rd(A1, B1, j, 3); mm(A0, B0); sched(); pstore(4 * j + 2);
        WAIT_L0(); BARS(17 + j);
        rd(A0, B0, j + 1, 0); mm(A1, B1); sched(); pstore(4 * j + 3);
      }
#pragma unroll
      for (int rg = 0; rg < 4; ++rg) {
        asm volatile("global_load_dwordx2 %0, %1, off" : "=v"(bv[rg]) : "v"(b2 + d0 + rg * 8) : "memory");
      }
#pragma unroll 1
      for (int j = 2; j < 10; ++j) {
        rd(A1, B1, j, 1); mm(A0, B0); sched();
        rd(A0, B0, j, 2); mm(A1, B1); sched();
        rd(A1, B1, j, 3); mm(A0, B0); sched();
        if (j == 3) {
          WAIT_V(0);
          if (lane == 0 && r != sg) __hip_atomic_store((gu64*)(ws + WS_FP) + (tile * 4 + r) * 8 + sg, tag, RLX_AGENT);
        }
        WAIT_L0(); BARS(17 + j);
        rd(A0, B0, j + 1, 0); mm(A1, B1); sched();
      }
      rd(A1, B1, 10, 1); mm(A0, B0); sched();
      rd(A0, B0, 10, 2); mm(A1, B1); sched();
      mm(A0, B0);
    }
    STAMP(8);
    WAIT_V(0);
#pragma unroll
    for (int rg = 0; rg < 4; ++rg) asm volatile("" : "+v"(bv[rg]));
    float gate[16];
#pragma unroll
    for (int rg = 0; rg < 4; ++rg) {
      const float bb[4] = {lo_f(bv[rg][0]), hi_f(bv[rg][0]), lo_f(bv[rg][1]), hi_f(bv[rg][1])};
#pragma unroll
      for (int j = 0; j < 4; ++j) {
        const float o2 = oc[4 * rg + j] + bb[j];
        gate[4 * rg + j] = o2 * sigm(o2);
      }
    }
#pragma unroll
    for (int j = 0; j < 16; j += 4) asm volatile("" : "+v"(gate[j]), "+v"(gate[j + 1]), "+v"(gate[j + 2]), "+v"(gate[j + 3]));
    BARS(27);
#pragma unroll
    for (int rg = 0; rg < 4; ++rg) {
      const u32 o = ((dh * 2 + t2) * 4 + rg) * 1024 + lane * 16;
      const f32x4 y0 = *(const LAS f32x4*)(L + MISC + o);
      const u32 ob = ((dh * 2 + t2) * 2 + (rg >> 1)) * 1024 + lane * 16 + (rg & 1) * 8;
      const u32x2 q1 = *(const LAS u32x2*)(L + 3 * BLK + ob), q2 = *(const LAS u32x2*)(L + 0 * BLK + ob), q3 = *(const LAS u32x2*)(L + 0 * BLK + 8192 + ob);
      LAS u32x2* xp = (LAS u32x2*)(L + 3 * BLK + 8192 + (t2 * 32 + l31) * 128 + (((dh * 4 + rg) ^ ((l31 >> 1) & 7)) << 4) + hh * 8);
      const u32x2 xv = *xp;
      const float ya[4] = {y0[0] + lo_f(q1[0]), y0[1] + hi_f(q1[0]), y0[2] + lo_f(q1[1]), y0[3] + hi_f(q1[1])};
      const float yb[4] = {lo_f(q2[0]) + lo_f(q3[0]), hi_f(q2[0]) + hi_f(q3[0]), lo_f(q2[1]) + lo_f(q3[1]), hi_f(q2[1]) + hi_f(q3[1])};
      const float xx[4] = {lo_f(xv[0]), hi_f(xv[0]), lo_f(xv[1]), hi_f(xv[1])};
      float res[4];
#pragma unroll
      for (int j = 0; j < 4; ++j) res[j] = xx[j] + (ya[j] + yb[j]) * gate[4 * rg + j];
      u32x2 ov = {pack2(res[0], res[1]), pack2(res[2], res[3])};
      *xp = ov;
    }
    WAIT_L0();
    BAR();
    STAMP(9);
  }
  {
    const int row = tid >> 3, ch = tid & 7;
    const u32x4 v = *(const LAS u32x4*)(L + 3 * BLK + 8192 + row * 128 + ch * 16);
    *(u32x4*)(out + (size_t)(stile * 128 + (sg >> 1) * 64 + row) * 512 + nt2 * 128 + (sg & 1) * 64 + ((ch ^ ((row >> 1) & 7)) << 3)) = v;
  }
  STAMP_FLUSH();
}

extern "C" void kernel_launch(void* const* d_in, const int* in_sizes, int n_in, void* d_out, int out_size, void* d_ws, size_t ws_size,
                              hipStream_t stream) {
  fused_k<<<256, 512, 0, stream>>>((const u16*)d_in[0], (const u16*)d_in[1], (const u16*)d_in[2], (const u16*)d_in[3], (const u16*)d_in[4],
                                   (const u16*)d_in[6], (const u16*)d_in[7], (const u16*)d_in[8], (const u16*)d_in[9], (u16*)d_out,
                                   (char*)d_ws);
}
```

```cpp
#include <hip/hip_runtime.h>
#include <hip/hip_bf16.h>
#include <stdint.h>

typedef unsigned short u16;
typedef unsigned int u32;
typedef unsigned long long u64;
typedef __attribute__((ext_vector_type(8))) short bf16x8;
typedef __attribute__((ext_vector_type(4))) short s16x4;
typedef __attribute__((ext_vector_type(2))) __bf16 bf2;
typedef __attribute__((ext_vector_type(4))) float f32x4;
typedef __attribute__((ext_vector_type(2))) float f32x2;
typedef __attribute__((ext_vector_type(16))) float f32x16;
typedef __attribute__((ext_vector_type(4))) unsigned u32x4;
typedef __attribute__((ext_vector_type(2))) unsigned u32x2;
#define LAS __attribute__((address_space(3)))
#define GAS __attribute__((address_space(1)))
typedef GAS u64 gu64;

#define PBF16 1
constexpr u32 WS_EPOCH = 0;
constexpr u32 WS_FAB = 4096;
constexpr u32 WS_FH = WS_FAB + 16384;
constexpr u32 WS_FP = WS_FH + 16384;
constexpr u32 WS_AB = 1u << 20;
constexpr u32 WS_H = WS_AB + (4u << 20);
constexpr u32 WS_P = WS_H + (6u << 20);
constexpr u32 WS_DBG = WS_P + (16u << 20);
constexpr u32 WS_END = WS_DBG + (2u << 20);

constexpr u32 BLK = 16384;
constexpr int CODE_LINES = 240;
constexpr u32 MISC = 9 * BLK;
constexpr u32 M_STAT = MISC;
constexpr u32 M_WS = MISC + 1024;
constexpr u32 M_CS = MISC + 2048;
constexpr u32 M_DUMMY = MISC + 4096;

#define RLX_AGENT __ATOMIC_RELAXED, __HIP_MEMORY_SCOPE_AGENT
#define WAIT_V(n) asm volatile("s_waitcnt vmcnt(" #n ")" ::: "memory")
#define WAIT_L0() asm volatile("s_waitcnt lgkmcnt(0)" ::: "memory")
#define BAR() __builtin_amdgcn_s_barrier()
#define SB0() __builtin_amdgcn_sched_barrier(0)
#define RFL(x) ((u32)__builtin_amdgcn_readfirstlane((u32)(x)))
#define BARS(i) BAR()
constexpr u32 SPIN_CAP = 1u << 20;

#define STAMP(i) do {} while (0)
#define STAMPD(i) do {} while (0)
#define STAMP_DECL
#define STAMP_FLUSH() do {} while (0)

__device__ __forceinline__ float rbf(float v) { return (float)(__bf16)v; }
__device__ __forceinline__ u32 pack2(float a, float b) { bf2 v = {(__bf16)a, (__bf16)b}; return __builtin_bit_cast(u32, v); }
__device__ __forceinline__ float lo_f(u32 v) { return __builtin_bit_cast(float, v << 16); }
__device__ __forceinline__ float hi_f(u32 v) { return __builtin_bit_cast(float, v & 0xffff0000u); }
__device__ __forceinline__ float sigm(float z) {
  return __builtin_amdgcn_rcpf(1.0f + __builtin_amdgcn_exp2f(-1.4426950408889634f * z));
}
__device__ __forceinline__ float gelu_t(float u) {
  const float z = u * (-2.3022081983f - 0.1029432398f * u * u);
  return u * __builtin_amdgcn_rcpf(1.0f + __builtin_amdgcn_exp2f(z));
}
__device__ __forceinline__ u32x4 mk_rs(const void* p, u32 bytes) {
  const u64 a = (u64)p;
  u32x4 r = {RFL(a), RFL((u32)(a >> 32) & 0xffffu), bytes, 0x00020000u};
  return r;
}
#define DMA4M(MOD, rs, d, v0, v1, v2, v3, so) do { u32 keep_; \
  asm volatile("s_nop 4\n\ts_mov_b32 %0, m0\n\ts_mov_b32 m0, %1\n\ts_nop 0\n\t" \
               "buffer_load_dwordx4 %2, %6, %7 offen " MOD " lds\n\t" \
               "buffer_load_dwordx4 %3, %6, %7 offen offset:1024 " MOD " lds\n\t" \
               "buffer_load_dwordx4 %4, %6, %7 offen offset:2048 " MOD " lds\n\t" \
               "buffer_load_dwordx4 %5, %6, %7 offen offset:3072 " MOD " lds\n\t" \
               "s_mov_b32 m0, %0" \
               : "=&s"(keep_) : "s"(RFL(d)), "v"(v0), "v"(v1), "v"(v2), "v"(v3), "s"(rs), "s"(RFL(so)) : "memory"); } while (0)
#define DMA4(rs, d, v0, v1, v2, v3, so) DMA4M("", rs, d, v0, v1, v2, v3, so)
#define DMA4S(rs, d, v0, v1, v2, v3, so) DMA4M("sc1", rs, d, v0, v1, v2, v3, so)
#define DMA2(rs, d, v0, v1, so) do { u32 keep_; \
  asm volatile("s_nop 4\n\ts_mov_b32 %0, m0\n\ts_mov_b32 m0, %1\n\ts_nop 0\n\t" \
               "buffer_load_dwordx4 %2, %4, %5 offen lds\n\t" \
               "buffer_load_dwordx4 %3, %4, %5 offen offset:1024 lds\n\t" \
               "s_mov_b32 m0, %0" \
               : "=&s"(keep_) : "s"(RFL(d)), "v"(v0), "v"(v1), "s"(rs), "s"(RFL(so)) : "memory"); } while (0)
#define TOUCH(rs, d, v0, so) do { u32 keep_; \
  asm volatile("s_nop 4\n\ts_mov_b32 %0, m0\n\ts_mov_b32 m0, %1\n\ts_nop 0\n\t" \
               "buffer_load_dword %2, %3, %4 offen lds\n\t" \
               "s_mov_b32 m0, %0" \
               : "=&s"(keep_) : "s"(RFL(d)), "v"(v0), "s"(rs), "s"(RFL(so)) : "memory"); } while (0)
#define TOUCHG(d, p) do { u32 keep_; \
  asm volatile("s_mov_b32 %0, m0\n\ts_mov_b32 m0, %1\n\ts_nop 0\n\t" \
               "global_load_lds_dword %2, off\n\t" \
               "s_mov_b32 m0, %0" \
               : "=&s"(keep_) : "s"(RFL(d)), "v"(p) : "memory"); } while (0)
#define MFMA(a, b, c) __builtin_amdgcn_mfma_f32_16x16x32_bf16(a, b, c, 0, 0, 0)
#define MFMA32(a, b, c) __builtin_amdgcn_mfma_f32_32x32x16_bf16(a, b, c, 0, 0, 0)

template <int NPER>
__device__ __forceinline__ void poll_flags(gu64* f, u64 tag, bool skip = false) {
  for (u32 sp = 0; sp < SPIN_CAP; ++sp) {
    bool ok = true;
#pragma unroll
    for (int k = 0; k < NPER; ++k) {
      u64 v = __hip_atomic_load(f + k * 64, RLX_AGENT);
      ok &= (v == tag);
    }
    if (__all(ok || skip)) break;
    __builtin_amdgcn_s_sleep(1);
  }
  asm volatile("" ::: "memory");
}
#define POLL_ISSUE(val, ptr) asm volatile("global_load_dwordx2 %0, %1, off sc1" : "=v"(val) : "v"(ptr) : "memory")

__global__ __launch_bounds__(512, 2) void fused_k(const u16* __restrict__ x, const u16* __restrict__ Cr, const u16* __restrict__ Ci,
                                                   const u16* __restrict__ Wr, const u16* __restrict__ Wi, const u16* __restrict__ w1,
                                                   const u16* __restrict__ b1, const u16* __restrict__ w2, const u16* __restrict__ b2,
                                                   u16* __restrict__ out, char* __restrict__ ws) {
  __shared__ __attribute__((aligned(1024))) char smem[163840];
  const int tid = threadIdx.x, lane = tid & 63;
  const int wave = __builtin_amdgcn_readfirstlane(tid >> 6);
  const int bid = blockIdx.x, xcd = bid & 7, idx = bid >> 3;
  const int stile = (xcd & 1) * 8 + (idx & 7);
  const int sg = xcd >> 1;
  const int nt2 = idx >> 3;
  const int cg = sg * 4 + nt2;
  const int g = wave >> 2, r = wave & 3;
  const int i16 = lane & 15, q = lane >> 4;
  LAS char* L = (LAS char*)smem;
  const u32 lb = (u32)(size_t)L;
  STAMP_DECL;
  STAMP(0);

  {
    const u64 pc0 = (u64)__builtin_amdgcn_s_getpc() & ~255ull;
    const int ln = idx * 8 + (lane & 7);
    if (wave == 7) TOUCHG(lb + MISC + 4096, (const char*)pc0 + (ln < CODE_LINES ? ln : 0) * 128);
  }
  const __amdgpu_buffer_rsrc_t ws_rs = __builtin_amdgcn_make_buffer_rsrc((void*)ws, 0, WS_END, 0x00020000);
  const u32x4 ws_r4 = mk_rs(ws, WS_END);
  const u32x4 c_rs = mk_rs((sg < 2 ? Cr : Ci) + (size_t)stile * 128 * 2048 + (sg & 1) * 1024, 128 * 4096);

  const int nh = cg < 11 ? 3 : 2;
  const int hu0 = cg < 11 ? 3 * cg : 33 + 2 * (cg - 11);
  const int hu2 = cg < 11 ? hu0 + 2 : hu0;
  u32x2 bvA[2], bvC[2];
#pragma unroll
  for (int ft = 0; ft < 2; ++ft) {
    asm volatile("global_load_dwordx2 %0, %1, off" : "=v"(bvA[ft]) : "v"(b1 + (hu0 + g) * 32 + ft * 16 + 4 * q) : "memory");
    asm volatile("global_load_dwordx2 %0, %1, off" : "=v"(bvC[ft]) : "v"(b1 + hu2 * 32 + ft * 16 + 4 * q) : "memory");
  }
  u64 tag;
  POLL_ISSUE(tag, (gu64*)(ws + WS_EPOCH));

  const u32x4 x_rs = mk_rs(x + (size_t)stile * 128 * 512, 131072);
  constexpr u32 SLOT = 24576;
  const u32x4 d_rs = wave < 4 ? x_rs : mk_rs((cg < 8 ? Wr : Wi) + (size_t)(cg & 7) * 64 * 512, 65536);
  int dv[4];
#pragma unroll
  for (int ii = 0; ii < 4; ++ii) dv[ii] = (ii * 8 + (lane >> 3)) * 1024 + (((lane & 7) ^ ((ii * 4 + (lane >> 4)) & 7)) << 4) - ii * 1024;
  const int rot1 = 0;
  auto issue1 = [&](int t) {
    if (wave < 6) DMA4(d_rs, lb + wave * 4096 + (t & 3) * SLOT, dv[0], dv[1], dv[2], dv[3], (wave & 3) * 32768 + ((t + rot1) & 7) * 128);
  };
  const u32x4 w1_rs = mk_rs(w1, 512 * 1376 * 2);
  constexpr int W1ST = 16 * 2752 - 1024;
  const int w1vo = (lane >> 2) * 2752 + (((((lane & 3) >> 1) ^ (lane >> 5)) & 1) << 5) + ((lane & 1) << 4);
  auto issue_hh = [&](int unit, int half, u32 blk) {
    if (wave >= 6) {
      const int p = half * 4 + (wave - 6) * 2;
      DMA4(w1_rs, lb + blk * BLK + (wave - 6) * 8192, w1vo, w1vo + W1ST, w1vo + 2 * W1ST, w1vo + 3 * W1ST, ((p + rot1) & 7) * 64 * 2752 + unit * 64);
      DMA4(w1_rs, lb + blk * BLK + (wave - 6) * 8192 + 4096, w1vo, w1vo + W1ST, w1vo + 2 * W1ST, w1vo + 3 * W1ST, ((p + 1 + rot1) & 7) * 64 * 2752 + unit * 64);
    }
  };
  issue1(0); BAR(); issue1(1); BAR(); issue1(2); issue1(3);
  issue_hh(hu0, 0, 6); issue_hh(hu0, 1, 7); issue_hh(hu0 + 1, 0, 8);
  STAMP(10);

  const bf16x8 ones = {0x3f80, 0x3f80, 0x3f80, 0x3f80, 0x3f80, 0x3f80, 0x3f80, 0x3f80};
  bf16x8 xf[2][16];
  f32x4 accA[2][2] = {};
  f32x4 sS = {}, sQ = {}, wsA[2] = {};
  {
    u32 xo[2][2], wo[2];
#pragma unroll
    for (int k = 0; k < 2; ++k) {
      const u32 sw = i16 * 128 + (((k * 4 + q) ^ (i16 >> 1)) << 4);
      xo[k][0] = r * 4096 + g * 2048 + sw; xo[k][1] = r * 4096 + (g ^ 1) * 2048 + sw;
      wo[k] = 16384 + g * 4096 + sw;
    }
    bf16x8 Wf[2][2][2];
    auto rd = [&](int t) {
      const u32 sb = (t & 3) * SLOT;
#pragma unroll
      for (int k = 0; k < 2; ++k) {
        xf[0][2 * t + k] = *(const LAS bf16x8*)(L + sb + xo[k][0]); xf[1][2 * t + k] = *(const LAS bf16x8*)(L + sb + xo[k][1]);
        Wf[t & 1][k][0] = *(const LAS bf16x8*)(L + sb + wo[k]); Wf[t & 1][k][1] = *(const LAS bf16x8*)(L + sb + wo[k] + 2048);
      }
    };
    if (wave < 6) WAIT_V(12); else WAIT_V(24);
    asm volatile("" : "+v"(bvA[0]), "+v"(bvA[1]), "+v"(bvC[0]), "+v"(bvC[1]), "+v"(tag));
    tag += 1ull;
    BAR();
    STAMP(1);
    rd(0);
    SB0();
#pragma unroll
    for (int t = 0; t < 8; ++t) {
      if (t < 7) {
        if (wave < 6) { if (t == 0) WAIT_V(8); else if (t < 5) WAIT_V(8); else if (t == 5) WAIT_V(4); else WAIT_V(0); }
        WAIT_L0();
        BAR();
        rd(t + 1);
      }
#pragma unroll
      for (int k = 0; k < 2; ++k) {
        const int ks = 2 * t + k;
#pragma unroll
        for (int nt = 0; nt < 2; ++nt) {
          accA[nt][0] = MFMA(xf[0][ks], Wf[t & 1][k][nt], accA[nt][0]);
          accA[nt][1] = MFMA(xf[1][ks], Wf[t & 1][k][nt], accA[nt][1]);
        }
        sS = MFMA(ones, xf[0][ks], sS);
        sQ = MFMA(xf[0][ks], xf[0][ks], sQ);
      }
      if (t < 7) {
#pragma unroll
        for (int i = 0; i < 8; ++i) {
          __builtin_amdgcn_sched_group_barrier(0x008, 1, 0);
          __builtin_amdgcn_sched_group_barrier(0x100, 1, 0);
        }
        __builtin_amdgcn_sched_group_barrier(0x008, 4, 0);
      }
      SB0();
      if (t < 4) issue1(t + 4);
      if (t == 4) issue_hh(hu0 + 1, 1, 0);
      if (t == 6) { issue_hh(hu2, 0, 2); issue_hh(hu2, 1, 3); }
      SB0();
      if (r == (t >> 1)) {
#pragma unroll
        for (int k = 0; k < 2; ++k) { wsA[0] = MFMA(ones, Wf[t & 1][k][0], wsA[0]); wsA[1] = MFMA(ones, Wf[t & 1][k][1], wsA[1]); }
      }
      SB0();
    }
  }
  {
    const int j = i16 & 3;
    float d = j == 0 ? sQ[0] : j == 1 ? sQ[1] : j == 2 ? sQ[2] : sQ[3];
    if (q == (i16 >> 2)) { f32x2 v = {sS[0], d}; *(LAS f32x2*)(L + M_STAT + (r * 32 + g * 16 + i16) * 8) = v; }
    if (q == 0) {
      *(LAS float*)(L + M_WS + ((g * 4 + r) * 32 + i16) * 4) = wsA[0][0];
      *(LAS float*)(L + M_WS + ((g * 4 + r) * 32 + 16 + i16) * 4) = wsA[1][0];
    }
  }
  if (wave >= 6) WAIT_V(16);
  WAIT_L0();
  BAR();
  STAMP(2);

  int cvo[4];
#pragma unroll
  for (int ii = 0; ii < 4; ++ii)
    cvo[ii] = ((r * 4 + ii) * 8 + (lane >> 3)) * 4096 + (((lane & 7) ^ (((ii & 1) * 4 + (lane >> 4)) & 7)) << 4) - ii * 1024;
  const int lv = lane * 16;
  auto blkC = [&](int st) -> u32 { return ((0x0541u >> ((st & 3) * 4)) & 15u) * BLK; };
  auto blkAB = [&](int st) -> u32 { return ((0x2876u >> ((st & 3) * 4)) & 15u) * BLK; };
  const int rot = 2 * (stile & 7) + (nt2 & 1) + 8 * (nt2 >> 1);
  auto issue_c = [&](int st) { DMA4(c_rs, lb + blkC(st) + r * 4096, cvo[0], cvo[1], cvo[2], cvo[3], ((st + rot) & 15) * 128); };
  const u32 ab_src = WS_AB + (((sg >> 1) * 4 + nt2) * 64 + (sg & 1) * 32) * 8192 + r * 4096;
  auto issue_ab = [&](int st) { DMA4S(ws_r4, lb + blkAB(st) + r * 4096, lv, lv, lv, lv, ab_src + ((st + rot) & 15) * 16384); SB0(); };

  const int c4 = (q & 1) * 2 + (q >> 1);
  float mH[2], rH[2];
  float mA[2], rA[2], mrow[2][4], rrow[2][4], wsv[2];
  auto epiA = [&](int ks) {
    if (ks == 0) {
#pragma unroll
      for (int mt = 0; mt < 2; ++mt) {
        const f32x2 sv = *(const LAS f32x2*)(L + M_STAT + (r * 32 + (mt ^ g) * 16 + i16) * 8);
        const float mean = sv[0] * (1.f / 512.f);
        const float var = fmaxf(sv[1] * (1.f / 512.f) - mean * mean, 0.f);
        const float mb = rbf(mean);
        const float den = rbf(__builtin_amdgcn_sqrtf(rbf(rbf(var) + 1e-5f)));
        const float inv = __builtin_amdgcn_rcpf(den);
        const float m2 = rbf((mean - mb) * inv);
        const float den2 = rbf(__builtin_amdgcn_sqrtf(rbf(rbf(var * inv * inv) + 1e-5f)));
        mA[mt] = mb; rA[mt] = cg < 8 ? inv : -inv;
        rH[mt] = inv * __builtin_amdgcn_rcpf(den2); mH[mt] = (mb + m2 * den) * rH[mt];
      }
    } else if (ks == 1) {
#pragma unroll
      for (int mt = 0; mt < 2; ++mt)
#pragma unroll
        for (int j = 0; j < 4; ++j) { mrow[mt][j] = __shfl(mA[mt], 4 * q + j); rrow[mt][j] = __shfl(rA[mt], 4 * q + j); }
#pragma unroll
      for (int nt = 0; nt < 2; ++nt) {
        wsv[nt] = 0.f;
#pragma unroll
        for (int rr = 0; rr < 4; ++rr) wsv[nt] += *(const LAS float*)(L + M_WS + ((g * 4 + rr) * 32 + nt * 16 + i16) * 4);
      }
    } else if (ks == 3 || ks == 5) {
      const int nt = ks == 5;
      const u32 dst = WS_AB + ((((cg >> 3) * 4 + ((cg & 7) >> 1)) * 64 + stile * 4 + r) * 8 + (cg & 1) * 4 + g * 2) * 1024 + ((c4 ^ (g * 2)) * 16 + i16) * 16;
      u32 p[2][2];
#pragma unroll
      for (int mt = 0; mt < 2; ++mt) {
        float v[4];
#pragma unroll
        for (int j = 0; j < 4; ++j) v[j] = (accA[nt][mt][j] - mrow[mt][j] * wsv[nt]) * rrow[mt][j];
        p[mt][0] = pack2(v[0], v[1]); p[mt][1] = pack2(v[2], v[3]);
      }
      auto s0 = __builtin_amdgcn_permlane16_swap(p[0][0], p[1][0], false, false);
      auto s1 = __builtin_amdgcn_permlane16_swap(p[0][1], p[1][1], false, false);
      u32x4 sv = {s0[0], s1[0], s0[1], s1[1]};
      __builtin_amdgcn_raw_buffer_store_b128(sv, ws_rs, dst + nt * 1024, 0, 16);
    }
  };
  epiA(0); epiA(1); epiA(3); epiA(5);
  SB0();
  STAMP(15);
  auto sideB = [&](int ks) {
    if (ks == 6) {
      WAIT_V(0);
      if (lane == 0)
        __hip_atomic_store((gu64*)(ws + WS_FAB) + (((((cg >> 3) * 4 + ((cg & 7) >> 1)) * 16 + stile) * 2 + (cg & 1)) * 8 + wave), tag, RLX_AGENT);
    } else if (ks == 8 || ks == 10 || ks == 12) {
      if (g == 0) issue_c((ks - 8) >> 1);
    }
  };

  auto h_mma = [&](f32x4 (&acc)[2][2], u32 d0, u32 d1, int slot, auto&& side) {
    const int a = i16 >> 2, p = lane & 3;
    u32 tb[2][2];
#pragma unroll
    for (int ft = 0; ft < 2; ++ft) {
      const u32 o = (8 * q + a) * 64 + ((ft ^ (q & 1)) * 32) + p * 8;
      tb[0][ft] = d0 + o; tb[1][ft] = d1 + o;
      acc[ft][0] = (f32x4){0.f, 0.f, 0.f, 0.f}; acc[ft][1] = (f32x4){0.f, 0.f, 0.f, 0.f};
    }
    {
      f32x4 cs[2] = {};
      const u32 cb = (r < 2 ? d0 : d1) + (r & 1) * 8192 - d0;
#pragma unroll
      for (int k = 0; k < 4; ++k)
#pragma unroll
        for (int ft = 0; ft < 2; ++ft) {
          s16x4 lo = __builtin_amdgcn_ds_read_tr16_b64_v4i16((LAS s16x4*)(L + tb[0][ft] + cb + k * 2048));
          s16x4 hi = __builtin_amdgcn_ds_read_tr16_b64_v4i16((LAS s16x4*)(L + tb[0][ft] + cb + k * 2048 + 256));
          cs[ft] = MFMA(__builtin_shufflevector(lo, hi, 0, 1, 2, 3, 4, 5, 6, 7), ones, cs[ft]);
        }
      if (i16 == 0) {
        *(LAS f32x4*)(L + M_CS + ((slot * 4 + r) * 32 + 4 * q) * 4) = cs[0];
        *(LAS f32x4*)(L + M_CS + ((slot * 4 + r) * 32 + 16 + 4 * q) * 4) = cs[1];
      }
    }
    bf16x8 wf[3][2];
    auto ldw = [&](int ks) {
#pragma unroll
      for (int ft = 0; ft < 2; ++ft) {
        s16x4 lo = __builtin_amdgcn_ds_read_tr16_b64_v4i16((LAS s16x4*)(L + tb[ks >> 3][ft] + (ks & 7) * 2048));
        s16x4 hi = __builtin_amdgcn_ds_read_tr16_b64_v4i16((LAS s16x4*)(L + tb[ks >> 3][ft] + (ks & 7) * 2048 + 256));
        wf[ks % 3][ft] = __builtin_shufflevector(lo, hi, 0, 1, 2, 3, 4, 5, 6, 7);
      }
    };
    ldw(0); ldw(1);
    SB0();
#pragma unroll
    for (int ks = 0; ks < 16; ++ks) {
      if (ks + 2 < 16) ldw(ks + 2);
#pragma unroll
      for (int ft = 0; ft < 2; ++ft) {
        acc[ft][0] = MFMA(wf[ks % 3][ft], xf[0][ks], acc[ft][0]);
        acc[ft][1] = MFMA(wf[ks % 3][ft], xf[1][ks], acc[ft][1]);
      }
      side(ks);
      SB0();
    }
  };
  auto preH = [&](f32x4 (&acc)[2][2], int slot, const u32x2* bv, u32 (&pa)[2][2][2]) {
#pragma unroll
    for (int ft = 0; ft < 2; ++ft) {
      f32x4 ecs = *(const LAS f32x4*)(L + M_CS + ((slot * 4 + 0) * 32 + ft * 16 + 4 * q) * 4);
#pragma unroll
      for (int rr = 1; rr < 4; ++rr) ecs += *(const LAS f32x4*)(L + M_CS + ((slot * 4 + rr) * 32 + ft * 16 + 4 * q) * 4);
      const float bj[4] = {lo_f(bv[ft][0]), hi_f(bv[ft][0]), lo_f(bv[ft][1]), hi_f(bv[ft][1])};
#pragma unroll
      for (int mt = 0; mt < 2; ++mt) {
        float v[4];
#pragma unroll
        for (int jj = 0; jj < 4; ++jj) v[jj] = acc[ft][mt][jj] * rH[mt] + (bj[jj] - mH[mt] * ecs[jj]);
        pa[mt][ft][0] = pack2(v[0], v[1]); pa[mt][ft][1] = pack2(v[2], v[3]);
      }
    }
  };
  auto actH = [&](u32 p00, u32 p01, u32 p10, u32 p11, int unit, int trow16) {
    const u32 dst = WS_H + (((stile * 2 + (r >> 1)) * 43 + unit) * 4 + (r & 1) * 2 + trow16) * 1024 + (c4 * 16 + i16) * 16;
    const u32 k00 = pack2(gelu_t(lo_f(p00)), gelu_t(hi_f(p00))), k01 = pack2(gelu_t(lo_f(p01)), gelu_t(hi_f(p01)));
    const u32 k10 = pack2(gelu_t(lo_f(p10)), gelu_t(hi_f(p10))), k11 = pack2(gelu_t(lo_f(p11)), gelu_t(hi_f(p11)));
    auto s0 = __builtin_amdgcn_permlane16_swap(k00, k10, false, false);
    auto s1 = __builtin_amdgcn_permlane16_swap(k01, k11, false, false);
    u32x4 sv = {s0[0], s1[0], s0[1], s1[1]};
    __builtin_amdgcn_raw_buffer_store_b128(sv, ws_rs, dst, 0, 16);
  };
  auto act2 = [&](u32 p) -> u32 { return pack2(gelu_t(lo_f(p)), gelu_t(hi_f(p))); };
  auto actH_fin = [&](u32 k00, u32 k01, u32 p10, u32 p11, int unit, int trow16) {
    const u32 dst = WS_H + (((stile * 2 + (r >> 1)) * 43 + unit) * 4 + (r & 1) * 2 + trow16) * 1024 + (c4 * 16 + i16) * 16;
    auto s0 = __builtin_amdgcn_permlane16_swap(k00, act2(p10), false, false);
    auto s1 = __builtin_amdgcn_permlane16_swap(k01, act2(p11), false, false);
    u32x4 sv = {s0[0], s1[0], s0[1], s1[1]};
    __builtin_amdgcn_raw_buffer_store_b128(sv, ws_rs, dst, 0, 16);
  };
  f32x4 accB[2][2], accC[2][2];
  h_mma(accB, (6 + 2 * g) * BLK, (g ? 0 : 7) * BLK, g, sideB);
  STAMP(14);
  WAIT_L0();
  BAR();
  STAMP(3);

  const int tile = stile * 4 + nt2;
  if (g == 1) {
    u32 pa[2][2][2];
    preH(accB, 1, bvA, pa);
    poll_flags<2>((gu64*)(ws + WS_FAB) + (((sg >> 1) * 4 + nt2) * 16 + (sg & 1) * 8) * 16 + lane, tag);
    STAMP(4);
    issue_ab(0); issue_ab(1); issue_ab(2); issue_c(3);
    const u32 h_src = WS_H + (stile * 2 + (sg >> 1)) * 43 * 4096;
    const u32x4 w2_rs = mk_rs(w2, 1376 * 512 * 2);
    int wvo[4];
#pragma unroll
    for (int ii = 0; ii < 4; ++ii) {
      const int c = lane & 7, f = ((lane >> 4) & 1) << 1;
      wvo[ii] = (ii * 8 + (lane >> 3)) * 1024 + (nt2 * 128 + (sg & 1) * 64) * 2 + ((((c >> 1) ^ f) << 5) | ((c & 1) << 4)) - ii * 1024;
    }
    const int orot = stile + nt2;
    auto issue_o = [&](int j) {
      int unit;
      if (j < 4) {
        const int c = ((j + orot) & 3) * 4 + r;
        unit = c < 11 ? 3 * c + 1 : 2 * c + 12;
      } else {
        int jj = j - 4 + (orot & 3) + ((orot >> 2) & 1); jj = jj >= 6 ? jj - 6 : jj; jj = j == 10 ? 6 : jj;
        const int t = min(jj * 4 + r, 26);
        unit = t < 22 ? 3 * (t >> 1) + 2 * (t & 1) : 2 * t - 11;
      }
      DMA4S(ws_r4, lb + blkC(j) + r * 4096, lv, lv, lv, lv, h_src + unit * 4096);
      DMA4(w2_rs, lb + blkAB(j) + r * 4096, wvo[0], wvo[1], wvo[2], wvo[3], unit * 32768);
      SB0();
    };
    STAMP(12);
    BAR();
    STAMP(13);
    issue_ab(3);
    actH(pa[0][0][0], pa[0][0][1], pa[0][1][0], pa[0][1][1], hu0 + 1, 1);
    actH(pa[1][0][0], pa[1][0][1], pa[1][1][0], pa[1][1][1], hu0 + 1, 0);
    SB0(); WAIT_V(18); BARS(0);
    STAMP(5);
    u32x4 pk_;
    u32 ka, kb;
    auto half_a = [&](int u, int mt) {
      pk_ = *(const LAS u32x4*)(L + 3 * BLK + ((u * 4 + r) * 2 + mt) * 1024 + lane * 16);
      ka = act2(pk_[0]); kb = act2(pk_[1]);
      asm volatile("" : "+v"(ka), "+v"(kb));
    };
    auto half_b = [&](int mt, int unit) { actH_fin(ka, kb, pk_[2], pk_[3], unit, mt); };
    SB0(); WAIT_V(14); BARS(1); issue_c(4); issue_ab(4);
    half_a(0, 0);
    SB0(); WAIT_V(8); BARS(2);
    if (lane == 0) __hip_atomic_store((gu64*)(ws + WS_FH) + ((stile * 4 + r) * 16 + cg), tag, RLX_AGENT);
    issue_c(5); issue_ab(5);
    half_b(0, hu0);
    SB0(); WAIT_V(16); BARS(3); issue_c(6); issue_ab(6);
    half_a(0, 1);
    SB0(); WAIT_V(16); BARS(4); issue_c(7); issue_ab(7);
    half_b(1, hu0);
    SB0(); WAIT_V(16); BARS(5); issue_c(8); issue_ab(8);
    if (nh == 3) half_a(1, 0);
    SB0(); WAIT_V(16); BARS(6); issue_c(9); issue_ab(9);
    if (nh == 3) half_b(0, hu2);
    SB0(); WAIT_V(16); BARS(7); issue_c(10); issue_ab(10);
    if (nh == 3) half_a(1, 1);
    SB0(); WAIT_V(16); BARS(8); issue_c(11); issue_ab(11);
    if (nh == 3) half_b(1, hu2);
    SB0(); WAIT_V(16); BARS(9); issue_c(12); issue_ab(12);
    {
      const int row = (stile & 7) * 172 + min(r * 64 + lane, 171);
      TOUCH(w2_rs, lb + 3 * BLK + r * 256, row * 1024 + (nt2 * 128 + (sg & 1) * 64) * 2, 0);
    }
    gu64* fhp = (gu64*)(ws + WS_FH) + (stile * 4 + 2 * (sg >> 1)) * 16 + (lane & 31);
    u64 pv;
    SB0(); WAIT_V(8); BARS(10);
    if (lane == 0) __hip_atomic_store((gu64*)(ws + WS_FH) + 1024 + ((stile * 4 + r) * 16 + cg), tag, RLX_AGENT);
    POLL_ISSUE(pv, fhp);
    issue_c(13); issue_ab(13);
    SB0(); WAIT_V(16); BARS(11); issue_c(14); issue_ab(14);
    SB0(); WAIT_V(16); BARS(12); issue_c(15); issue_ab(15);
    asm volatile("" : "+v"(pv));
    if (!__all(pv == tag)) poll_flags<1>(fhp, tag);
    SB0(); WAIT_V(16); BARS(13); issue_o(0);
    SB0(); WAIT_V(16); BARS(14); POLL_ISSUE(pv, fhp + 1024); issue_o(1);
    SB0(); WAIT_V(16); BARS(15); issue_o(2);
    SB0(); WAIT_V(16); BARS(16); issue_o(3);
    asm volatile("" : "+v"(pv));
    if (!__all(pv == tag)) poll_flags<1>(fhp + 1024, tag);
#pragma unroll 1
    for (int i = 17; i < 21; ++i) { WAIT_V(16); BARS(i); issue_o(i - 13); }
    gu64* fpp = (gu64*)(ws + WS_FP) + (tile * 4 + sg) * 8 + (lane & 3);
    gu64* fall = (gu64*)(ws + WS_FAB) + ((bid == 0 && r == 0) ? lane * 8 : 0);
    u64 e0, e1, e2, e3;
    SB0(); WAIT_V(16); BARS(21);
    POLL_ISSUE(e0, fall); POLL_ISSUE(e1, fall + 512); POLL_ISSUE(e2, fall + 1024); POLL_ISSUE(e3, fall + 1536);
    issue_o(8);
    SB0(); WAIT_V(16); BARS(22); POLL_ISSUE(pv, fpp); issue_o(9);
    SB0(); WAIT_V(16); BARS(23); issue_o(10);
    const u32 p_src = WS_P + (tile * 4 + sg) * 65536 + r * 4096;
    SB0(); WAIT_V(16); BARS(24);
    asm volatile("" : "+v"(pv));
    if (!__all(pv == tag || (lane & 3) == sg)) poll_flags<1>(fpp, tag, (lane & 3) == sg);
    DMA4S(ws_r4, lb + blkC(3) + r * 4096, lv, lv, lv, lv, WS_P + (tile * 4 + sg) * 65536 + (sg ^ (2 + (r >> 1))) * 16384 + (r & 1) * 4096);
    if (r < 2) {
      DMA4S(ws_r4, lb + 3 * BLK + r * 4096, lv, lv, lv, lv, WS_P + (tile * 4 + sg) * 65536 + (sg ^ 1) * 16384 + r * 4096);
    } else {
      int xo[4];
#pragma unroll
      for (int ii = 0; ii < 4; ++ii) xo[ii] = (ii * 8 + (lane >> 3)) * 1024 + (((lane & 7) ^ ((ii * 4 + (lane >> 4)) & 7)) << 4) - ii * 1024;
      DMA4(x_rs, lb + 3 * BLK + r * 4096, xo[0], xo[1], xo[2], xo[3], ((sg >> 1) * 64 + (r - 2) * 32) * 1024 + (nt2 * 128 + (sg & 1) * 64) * 2);
    }
    SB0(); WAIT_V(16); BARS(25);
    SB0(); WAIT_V(8); BARS(26);
    SB0(); WAIT_V(0); BARS(27);
    if (bid == 0 && r == 0) {
      asm volatile("" : "+v"(e0), "+v"(e1), "+v"(e2), "+v"(e3));
      if (!__all(e0 == tag && e1 == tag && e2 == tag && e3 == tag)) {
        for (u32 sp = 0; sp < SPIN_CAP; ++sp) {
          bool ok = true;
#pragma unroll
          for (int k = 0; k < 4; ++k) ok &= __hip_atomic_load(fall + k * 512, RLX_AGENT) == tag;
          if (__all(ok)) break;
          __builtin_amdgcn_s_sleep(1);
        }
      }
      if (lane == 0) __hip_atomic_store((gu64*)(ws + WS_EPOCH), tag, RLX_AGENT);
    }
    BAR();
    STAMP(6);
  } else {
    u32 paB[2][2][2];
    preH(accB, 0, bvA, paB);
    STAMP(11);
    if (nh == 3) h_mma(accC, 2 * BLK, 3 * BLK, 2, [&](int) {});
    WAIT_L0();
    STAMP(12);
    BAR();
    STAMP(13);
#pragma unroll
    for (int mt = 0; mt < 2; ++mt) {
      u32x4 v = {paB[mt][0][0], paB[mt][0][1], paB[mt][1][0], paB[mt][1][1]};
      *(LAS u32x4*)(L + 3 * BLK + (r * 2 + mt) * 1024 + lane * 16) = v;
    }
    if (nh == 3) {
      u32 paC[2][2][2];
      preH(accC, 2, bvC, paC);
#pragma unroll
      for (int mt = 0; mt < 2; ++mt) {
        u32x4 v = {paC[mt][0][0], paC[mt][0][1], paC[mt][1][0], paC[mt][1][1]};
        *(LAS u32x4*)(L + 3 * BLK + ((4 + r) * 2 + mt) * 1024 + lane * 16) = v;
      }
    }
    STAMP(4);
    const int th = r >> 1, eh = r & 1;
    const int l31 = lane & 31, hh = lane >> 5;
    f32x16 acc[2][2] = {};
    {
      const u32 a_off = eh * 4096 + ((lane >> 4) & 1) * 1024 + hh * 256 + i16 * 16;
      u32 c_off[4];
#pragma unroll
      for (int k4 = 0; k4 < 4; ++k4) c_off[k4] = (th * 64 + l31) * 128 + (((k4 * 2 + hh) ^ ((l31 >> 1) & 7)) << 4);
      bf16x8 A0[2][2], B0[2][2], A1[2][2], B1[2][2];
      auto rd = [&](bf16x8 (*A)[2], bf16x8 (*B)[2], int st, int kh) {
        LAS char* sa = L + blkAB(st) + a_off;
        LAS char* sc = L + blkC(st);
#pragma unroll
        for (int k2 = 0; k2 < 2; ++k2)
#pragma unroll
          for (int e = 0; e < 2; ++e) {
            A[k2][e] = *(const LAS bf16x8*)(sa + kh * 8192 + e * 2048 + k2 * 512);
            B[k2][e] = *(const LAS bf16x8*)(sc + c_off[kh * 2 + k2] + e * 4096);
          }
      };
      auto mm = [&](bf16x8 (*A)[2], bf16x8 (*B)[2]) {
#pragma unroll
        for (int k2 = 0; k2 < 2; ++k2)
#pragma unroll
          for (int e = 0; e < 2; ++e)
#pragma unroll
            for (int t = 0; t < 2; ++t) acc[e][t] = MFMA32(A[k2][e], B[k2][t], acc[e][t]);
      };
      auto sched = [&]() {
#pragma unroll
        for (int i = 0; i < 4; ++i) {
          __builtin_amdgcn_sched_group_barrier(0x008, 1, 0);
          __builtin_amdgcn_sched_group_barrier(0x100, 2, 0);
        }
        __builtin_amdgcn_sched_group_barrier(0x008, 4, 0);
      };
      __builtin_amdgcn_s_setprio(2);
      WAIT_V(0); WAIT_L0(); BARS(0);
      STAMP(5);
      rd(A0, B0, 0, 0);
#pragma unroll 1
      for (int st = 0; st < 15; ++st) {
        rd(A1, B1, st, 1);
        mm(A0, B0);
        sched();
        WAIT_L0(); BARS(st + 1);
        rd(A0, B0, st + 1, 0);
        mm(A1, B1);
        sched();
        SB0();
      }
      rd(A1, B1, 15, 1);
      mm(A0, B0);
      sched();
      WAIT_L0(); BARS(16);
      mm(A1, B1);
    }
    STAMP(6);
    if (r == sg) {
#pragma unroll
      for (int e = 0; e < 2; ++e)
#pragma unroll
        for (int t = 0; t < 2; ++t)
#pragma unroll
          for (int rg = 0; rg < 4; ++rg) {
            f32x4 v = {acc[e][t][4 * rg], acc[e][t][4 * rg + 1], acc[e][t][4 * rg + 2], acc[e][t][4 * rg + 3]};
            *(LAS f32x4*)(L + MISC + ((e * 2 + t) * 4 + rg) * 1024 + lane * 16) = v;
          }
    }
    const u32 pdst = WS_P + ((tile * 4 + r) * 4 + sg) * 16384 + lane * 16;
    auto pstore = [&](int i) {
      if (r != sg) {
        {
          const int e = i >> 2, t = (i >> 1) & 1, h2 = i & 1;
          u32x4 v = {pack2(acc[e][t][8 * h2], acc[e][t][8 * h2 + 1]), pack2(acc[e][t][8 * h2 + 2], acc[e][t][8 * h2 + 3]),
                     pack2(acc[e][t][8 * h2 + 4], acc[e][t][8 * h2 + 5]), pack2(acc[e][t][8 * h2 + 6], acc[e][t][8 * h2 + 7])};
          __builtin_amdgcn_raw_buffer_store_b128(v, ws_rs, pdst + i * 1024, 0, 16);
        }
        if (false)
#pragma unroll
        for (int u = 2 * i; u < 2 * i + 2; ++u) {
          const int e = u >> 3, t = (u >> 2) & 1, rg = u & 3;
          f32x4 v = {acc[e][t][4 * rg], acc[e][t][4 * rg + 1], acc[e][t][4 * rg + 2], acc[e][t][4 * rg + 3]};
          __builtin_amdgcn_raw_buffer_store_b128(__builtin_bit_cast(u32x4, v), ws_rs, pdst + u * 1024, 0, 16);
        }
      }
    };
    STAMP(7);
    const int dh = r & 1, t2 = r >> 1;
    const int d0 = nt2 * 128 + (sg & 1) * 64 + dh * 32 + 4 * hh;
    f32x16 oc = {};
    u32x2 bv[4];
    {
      const int a = i16 >> 2, p = lane & 3;
      const u32 wb = (8 * hh + a) * 128 + (((dh * 2 + ((lane >> 4) & 1)) ^ (a & 2)) * 32) + p * 8;
      const u32 hb = t2 * 2048 + ((lane >> 4) & 1) * 1024 + hh * 256 + i16 * 16;
      bf16x8 A0[2], B0[2], A1[2], B1[2];
      auto rd = [&](bf16x8* A, bf16x8* B, int j, int kk) {
        LAS char* sw2 = L + blkAB(j) + wb;
        LAS char* sh = L + blkC(j) + hb;
#pragma unroll
        for (int k2 = 0; k2 < 2; ++k2) {
          s16x4 lo = __builtin_amdgcn_ds_read_tr16_b64_v4i16((LAS s16x4*)(sw2 + kk * 4096 + k2 * 2048));
          s16x4 hi = __builtin_amdgcn_ds_read_tr16_b64_v4i16((LAS s16x4*)(sw2 + kk * 4096 + k2 * 2048 + 512));
          A[k2] = __builtin_shufflevector(lo, hi, 0, 1, 2, 3, 4, 5, 6, 7);
          B[k2] = *(const LAS bf16x8*)(sh + kk * 4096 + k2 * 512);
        }
      };
      auto mm = [&](bf16x8* A, bf16x8* B) {
        oc = MFMA32(A[0], B[0], oc);
        oc = MFMA32(A[1], B[1], oc);
      };
      auto sched = [&]() {
        __builtin_amdgcn_sched_group_barrier(0x100, 3, 0);
        __builtin_amdgcn_sched_group_barrier(0x008, 1, 0);
        __builtin_amdgcn_sched_group_barrier(0x100, 3, 0);
        __builtin_amdgcn_sched_group_barrier(0x008, 1, 0);
        SB0();
      };
      rd(A0, B0, 0, 0);
      SB0();
#pragma unroll
      for (int j = 0; j < 2; ++j) {
        rd(A1, B1, j, 1); mm(A0, B0); sched(); pstore(4 * j);
        rd(A0, B0, j, 2); mm(A1, B1); sched(); pstore(4 * j + 1);
        rd(A1, B1, j, 3); mm(A0, B0); sched(); pstore(4 * j + 2);
        WAIT_L0(); BARS(17 + j);
        rd(A0, B0, j + 1, 0); mm(A1, B1); sched(); pstore(4 * j + 3);
      }
#pragma unroll
      for (int rg = 0; rg < 4; ++rg) {
        asm volatile("global_load_dwordx2 %0, %1, off" : "=v"(bv[rg]) : "v"(b2 + d0 + rg * 8) : "memory");
      }
#pragma unroll 1
      for (int j = 2; j < 10; ++j) {
        rd(A1, B1, j, 1); mm(A0, B0); sched();
        rd(A0, B0, j, 2); mm(A1, B1); sched();
        rd(A1, B1, j, 3); mm(A0, B0); sched();
        if (j == 3) {
          WAIT_V(0);
          if (lane == 0 && r != sg) __hip_atomic_store((gu64*)(ws + WS_FP) + (tile * 4 + r) * 8 + sg, tag, RLX_AGENT);
        }
        WAIT_L0(); BARS(17 + j);
        rd(A0, B0, j + 1, 0); mm(A1, B1); sched();
      }
      rd(A1, B1, 10, 1); mm(A0, B0); sched();
      rd(A0, B0, 10, 2); mm(A1, B1); sched();
      mm(A0, B0);
    }
    STAMP(8);
    WAIT_V(0);
#pragma unroll
    for (int rg = 0; rg < 4; ++rg) asm volatile("" : "+v"(bv[rg]));
    float gate[16];
#pragma unroll
    for (int rg = 0; rg < 4; ++rg) {
      const float bb[4] = {lo_f(bv[rg][0]), hi_f(bv[rg][0]), lo_f(bv[rg][1]), hi_f(bv[rg][1])};
#pragma unroll
      for (int j = 0; j < 4; ++j) {
        const float o2 = oc[4 * rg + j] + bb[j];
        gate[4 * rg + j] = o2 * sigm(o2);
      }
    }
#pragma unroll
    for (int j = 0; j < 16; j += 4) asm volatile("" : "+v"(gate[j]), "+v"(gate[j + 1]), "+v"(gate[j + 2]), "+v"(gate[j + 3]));
    BARS(27);
#pragma unroll
    for (int rg = 0; rg < 4; ++rg) {
      const u32 o = ((dh * 2 + t2) * 4 + rg) * 1024 + lane * 16;
      const f32x4 y0 = *(const LAS f32x4*)(L + MISC + o);
      const u32 ob = ((dh * 2 + t2) * 2 + (rg >> 1)) * 1024 + lane * 16 + (rg & 1) * 8;
      const u32x2 q1 = *(const LAS u32x2*)(L + 3 * BLK + ob), q2 = *(const LAS u32x2*)(L + 0 * BLK + ob), q3 = *(const LAS u32x2*)(L + 0 * BLK + 8192 + ob);
      LAS u32x2* xp = (LAS u32x2*)(L + 3 * BLK + 8192 + (t2 * 32 + l31) * 128 + (((dh * 4 + rg) ^ ((l31 >> 1) & 7)) << 4) + hh * 8);
      const u32x2 xv = *xp;
      const float ya[4] = {y0[0] + lo_f(q1[0]), y0[1] + hi_f(q1[0]), y0[2] + lo_f(q1[1]), y0[3] + hi_f(q1[1])};
      const float yb[4] = {lo_f(q2[0]) + lo_f(q3[0]), hi_f(q2[0]) + hi_f(q3[0]), lo_f(q2[1]) + lo_f(q3[1]), hi_f(q2[1]) + hi_f(q3[1])};
      const float xx[4] = {lo_f(xv[0]), hi_f(xv[0]), lo_f(xv[1]), hi_f(xv[1])};
      float res[4];
#pragma unroll
      for (int j = 0; j < 4; ++j) res[j] = xx[j] + (ya[j] + yb[j]) * gate[4 * rg + j];
      u32x2 ov = {pack2(res[0], res[1]), pack2(res[2], res[3])};
      *xp = ov;
    }
    WAIT_L0();
    BAR();
    STAMP(9);
  }
  {
    const int row = tid >> 3, ch = tid & 7;
    const u32x4 v = *(const LAS u32x4*)(L + 3 * BLK + 8192 + row * 128 + ch * 16);
    *(u32x4*)(out + (size_t)(stile * 128 + (sg >> 1) * 64 + row) * 512 + nt2 * 128 + (sg & 1) * 64 + ((ch ^ ((row >> 1) & 7)) << 3)) = v;
  }
  STAMP_FLUSH();
}

extern "C" void kernel_launch(void* const* d_in, const int* in_sizes, int n_in, void* d_out, int out_size, void* d_ws, size_t ws_size,
                              hipStream_t stream) {
  fused_k<<<256, 512, 0, stream>>>((const u16*)d_in[0], (const u16*)d_in[1], (const u16*)d_in[2], (const u16*)d_in[3], (const u16*)d_in[4],
                                   (const u16*)d_in[6], (const u16*)d_in[7], (const u16*)d_in[8], (const u16*)d_in[9], (u16*)d_out,
                                   (char*)d_ws);
}
```

```cpp
#include <hip/hip_runtime.h>
#include <hip/hip_bf16.h>
#include <stdint.h>

typedef unsigned short u16;
typedef unsigned int u32;
typedef unsigned long long u64;
typedef __attribute__((ext_vector_type(8))) short bf16x8;
typedef __attribute__((ext_vector_type(4))) short s16x4;
typedef __attribute__((ext_vector_type(2))) __bf16 bf2;
typedef __attribute__((ext_vector_type(4))) float f32x4;
typedef __attribute__((ext_vector_type(2))) float f32x2;
typedef __attribute__((ext_vector_type(16))) float f32x16;
typedef __attribute__((ext_vector_type(4))) unsigned u32x4;
typedef __attribute__((ext_vector_type(2))) unsigned u32x2;
#define LAS __attribute__((address_space(3)))
#define GAS __attribute__((address_space(1)))
typedef GAS u64 gu64;

constexpr u32 WS_EPOCH = 0;
constexpr u32 WS_FAB = 4096;
constexpr u32 WS_FH = WS_FAB + 16384;
constexpr u32 WS_FP = WS_FH + 16384;
constexpr u32 WS_AB = 1u << 20;
constexpr u32 WS_H = WS_AB + (4u << 20);
constexpr u32 WS_P = WS_H + (6u << 20);
constexpr u32 WS_END = WS_P + (16u << 20);

constexpr u32 BLK = 16384;
constexpr int CODE_LINES = 240;
constexpr u32 MISC = 9 * BLK;
constexpr u32 M_STAT = MISC;
constexpr u32 M_WS = MISC + 1024;
constexpr u32 M_CS = MISC + 2048;

#define RLX_AGENT __ATOMIC_RELAXED, __HIP_MEMORY_SCOPE_AGENT
#define WAIT_V(n) asm volatile("s_waitcnt vmcnt(" #n ")" ::: "memory")
#define WAIT_L0() asm volatile("s_waitcnt lgkmcnt(0)" ::: "memory")
#define BAR() __builtin_amdgcn_s_barrier()
#define SB0() __builtin_amdgcn_sched_barrier(0)
#define RFL(x) ((u32)__builtin_amdgcn_readfirstlane((u32)(x)))
constexpr u32 SPIN_CAP = 1u << 18;


__device__ __forceinline__ float rbf(float v) { return (float)(__bf16)v; }
__device__ __forceinline__ u32 pack2(float a, float b) { bf2 v = {(__bf16)a, (__bf16)b}; return __builtin_bit_cast(u32, v); }
__device__ __forceinline__ float lo_f(u32 v) { return __builtin_bit_cast(float, v << 16); }
__device__ __forceinline__ float hi_f(u32 v) { return __builtin_bit_cast(float, v & 0xffff0000u); }
__device__ __forceinline__ float sigm(float z) {
  return __builtin_amdgcn_rcpf(1.0f + __builtin_amdgcn_exp2f(-1.4426950408889634f * z));
}
__device__ __forceinline__ float gelu_t(float u) {
  const float z = u * (-2.3022081983f - 0.1029432398f * u * u);
  return u * __builtin_amdgcn_rcpf(1.0f + __builtin_amdgcn_exp2f(z));
}
__device__ __forceinline__ u32x4 mk_rs(const void* p, u32 bytes) {
  const u64 a = (u64)p;
  u32x4 r = {RFL(a), RFL((u32)(a >> 32) & 0xffffu), bytes, 0x00020000u};
  return r;
}
#define DMA4M(MOD, rs, d, v0, v1, v2, v3, so) do { u32 keep_; \
  asm volatile("s_nop 4\n\ts_mov_b32 %0, m0\n\ts_mov_b32 m0, %1\n\ts_nop 0\n\t" \
               "buffer_load_dwordx4 %2, %6, %7 offen " MOD " lds\n\t" \
               "buffer_load_dwordx4 %3, %6, %7 offen offset:1024 " MOD " lds\n\t" \
               "buffer_load_dwordx4 %4, %6, %7 offen offset:2048 " MOD " lds\n\t" \
               "buffer_load_dwordx4 %5, %6, %7 offen offset:3072 " MOD " lds\n\t" \
               "s_mov_b32 m0, %0" \
               : "=&s"(keep_) : "s"(RFL(d)), "v"(v0), "v"(v1), "v"(v2), "v"(v3), "s"(rs), "s"(RFL(so)) : "memory"); } while (0)
#define DMA4(rs, d, v0, v1, v2, v3, so) DMA4M("", rs, d, v0, v1, v2, v3, so)
#define DMA4S(rs, d, v0, v1, v2, v3, so) DMA4M("sc1", rs, d, v0, v1, v2, v3, so)
#define TOUCH(rs, d, v0, so) do { u32 keep_; \
  asm volatile("s_nop 4\n\ts_mov_b32 %0, m0\n\ts_mov_b32 m0, %1\n\ts_nop 0\n\t" \
               "buffer_load_dword %2, %3, %4 offen lds\n\t" \
               "s_mov_b32 m0, %0" \
               : "=&s"(keep_) : "s"(RFL(d)), "v"(v0), "s"(rs), "s"(RFL(so)) : "memory"); } while (0)
#define TOUCHG(d, p) do { u32 keep_; \
  asm volatile("s_mov_b32 %0, m0\n\ts_mov_b32 m0, %1\n\ts_nop 0\n\t" \
               "global_load_lds_dword %2, off\n\t" \
               "s_mov_b32 m0, %0" \
               : "=&s"(keep_) : "s"(RFL(d)), "v"(p) : "memory"); } while (0)
#define MFMA(a, b, c) __builtin_amdgcn_mfma_f32_16x16x32_bf16(a, b, c, 0, 0, 0)
#define MFMA32(a, b, c) __builtin_amdgcn_mfma_f32_32x32x16_bf16(a, b, c, 0, 0, 0)

template <int NPER>
__device__ __forceinline__ void poll_flags(gu64* f, u64 tag, bool skip = false) {
  for (u32 sp = 0; sp < SPIN_CAP; ++sp) {
    bool ok = true;
#pragma unroll
    for (int k = 0; k < NPER; ++k) {
      u64 v = __hip_atomic_load(f + k * 64, RLX_AGENT);
      ok &= (v == tag);
    }
    if (__all(ok || skip)) break;
    __builtin_amdgcn_s_sleep(1);
  }
  asm volatile("" ::: "memory");
}
#define POLL_ISSUE(val, ptr) asm volatile("global_load_dwordx2 %0, %1, off sc1" : "=v"(val) : "v"(ptr) : "memory")

__global__ __launch_bounds__(512, 2) void fused_k(const u16* __restrict__ x, const u16* __restrict__ Cr, const u16* __restrict__ Ci,
                                                   const u16* __restrict__ Wr, const u16* __restrict__ Wi, const u16* __restrict__ w1,
                                                   const u16* __restrict__ b1, const u16* __restrict__ w2, const u16* __restrict__ b2,
                                                   u16* __restrict__ out, char* __restrict__ ws) {
  __shared__ __attribute__((aligned(1024))) char smem[163840];
  const int tid = threadIdx.x, lane = tid & 63;
  const int wave = __builtin_amdgcn_readfirstlane(tid >> 6);
  const int bid = blockIdx.x, xcd = bid & 7, idx = bid >> 3;
  const int stile = (xcd & 1) * 8 + (idx & 7);
  const int sg = xcd >> 1;
  const int nt2 = idx >> 3;
  const int cg = sg * 4 + nt2;
  const int g = wave >> 2, r = wave & 3;
  const int i16 = lane & 15, q = lane >> 4;
  LAS char* L = (LAS char*)smem;
  const u32 lb = (u32)(size_t)L;

  {
    const u64 pc0 = (u64)__builtin_amdgcn_s_getpc() & ~255ull;
    const int ln = idx * 8 + (lane & 7);
    if (wave == 7) TOUCHG(lb + MISC + 4096, (const char*)pc0 + (ln < CODE_LINES ? ln : 0) * 128);
  }
  const __amdgpu_buffer_rsrc_t ws_rs = __builtin_amdgcn_make_buffer_rsrc((void*)ws, 0, WS_END, 0x00020000);
  const u32x4 ws_r4 = mk_rs(ws, WS_END);
  const u32x4 c_rs = mk_rs((sg < 2 ? Cr : Ci) + (size_t)stile * 128 * 2048 + (sg & 1) * 1024, 128 * 4096);

  const int nh = cg < 11 ? 3 : 2;
  const int hu0 = cg < 11 ? 3 * cg : 33 + 2 * (cg - 11);
  const int hu2 = cg < 11 ? hu0 + 2 : hu0;
  u32x2 bvA[2], bvC[2];
#pragma unroll
  for (int ft = 0; ft < 2; ++ft) {
    asm volatile("global_load_dwordx2 %0, %1, off" : "=v"(bvA[ft]) : "v"(b1 + (hu0 + g) * 32 + ft * 16 + 4 * q) : "memory");
    asm volatile("global_load_dwordx2 %0, %1, off" : "=v"(bvC[ft]) : "v"(b1 + hu2 * 32 + ft * 16 + 4 * q) : "memory");
  }
  u64 tag;
  POLL_ISSUE(tag, (gu64*)(ws + WS_EPOCH));

  const u32x4 x_rs = mk_rs(x + (size_t)stile * 128 * 512, 131072);
  constexpr u32 SLOT = 24576;
  const u32x4 d_rs = wave < 4 ? x_rs : mk_rs((cg < 8 ? Wr : Wi) + (size_t)(cg & 7) * 64 * 512, 65536);
  int dv[4];
#pragma unroll
  for (int ii = 0; ii < 4; ++ii) dv[ii] = (ii * 8 + (lane >> 3)) * 1024 + (((lane & 7) ^ ((ii * 4 + (lane >> 4)) & 7)) << 4) - ii * 1024;
  auto issue1 = [&](int t) {
    if (wave < 6) DMA4(d_rs, lb + wave * 4096 + (t & 3) * SLOT, dv[0], dv[1], dv[2], dv[3], (wave & 3) * 32768 + t * 128);
  };
  const u32x4 w1_rs = mk_rs(w1, 512 * 1376 * 2);
  constexpr int W1ST = 16 * 2752 - 1024;
  const int w1vo = (lane >> 2) * 2752 + (((((lane & 3) >> 1) ^ (lane >> 5)) & 1) << 5) + ((lane & 1) << 4);
  auto issue_hh = [&](int unit, int half, u32 blk) {
    if (wave >= 6) {
      const int p = half * 4 + (wave - 6) * 2;
      DMA4(w1_rs, lb + blk * BLK + (wave - 6) * 8192, w1vo, w1vo + W1ST, w1vo + 2 * W1ST, w1vo + 3 * W1ST, p * 64 * 2752 + unit * 64);
      DMA4(w1_rs, lb + blk * BLK + (wave - 6) * 8192 + 4096, w1vo, w1vo + W1ST, w1vo + 2 * W1ST, w1vo + 3 * W1ST, (p + 1) * 64 * 2752 + unit * 64);
    }
  };
  issue1(0); BAR(); issue1(1); BAR(); issue1(2); issue1(3);
  issue_hh(hu0, 0, 6); issue_hh(hu0, 1, 7); issue_hh(hu0 + 1, 0, 8);

  const bf16x8 ones = {0x3f80, 0x3f80, 0x3f80, 0x3f80, 0x3f80, 0x3f80, 0x3f80, 0x3f80};
  bf16x8 xf[2][16];
  f32x4 accA[2][2] = {};
  f32x4 sS = {}, sQ = {}, wsA[2] = {};
  {
    u32 xo[2][2], wo[2];
#pragma unroll
    for (int k = 0; k < 2; ++k) {
      const u32 sw = i16 * 128 + (((k * 4 + q) ^ (i16 >> 1)) << 4);
      xo[k][0] = r * 4096 + g * 2048 + sw; xo[k][1] = r * 4096 + (g ^ 1) * 2048 + sw;
      wo[k] = 16384 + g * 4096 + sw;
    }
    bf16x8 Wf[2][2][2];
    auto rd = [&](int t) {
      const u32 sb = (t & 3) * SLOT;
#pragma unroll
      for (int k = 0; k < 2; ++k) {
        xf[0][2 * t + k] = *(const LAS bf16x8*)(L + sb + xo[k][0]); xf[1][2 * t + k] = *(const LAS bf16x8*)(L + sb + xo[k][1]);
        Wf[t & 1][k][0] = *(const LAS bf16x8*)(L + sb + wo[k]); Wf[t & 1][k][1] = *(const LAS bf16x8*)(L + sb + wo[k] + 2048);
      }
    };
    if (wave < 6) WAIT_V(12); else WAIT_V(24);
    asm volatile("" : "+v"(bvA[0]), "+v"(bvA[1]), "+v"(bvC[0]), "+v"(bvC[1]), "+v"(tag));
    tag += 1ull;
    BAR();
    rd(0);
    SB0();
#pragma unroll
    for (int t = 0; t < 8; ++t) {
      if (t < 7) {
        if (wave < 6) { if (t < 5) WAIT_V(8); else if (t == 5) WAIT_V(4); else WAIT_V(0); }
        WAIT_L0();
        BAR();
        rd(t + 1);
      }
#pragma unroll
      for (int k = 0; k < 2; ++k) {
        const int ks = 2 * t + k;
#pragma unroll
        for (int nt = 0; nt < 2; ++nt) {
          accA[nt][0] = MFMA(xf[0][ks], Wf[t & 1][k][nt], accA[nt][0]);
          accA[nt][1] = MFMA(xf[1][ks], Wf[t & 1][k][nt], accA[nt][1]);
        }
        sS = MFMA(ones, xf[0][ks], sS);
        sQ = MFMA(xf[0][ks], xf[0][ks], sQ);
      }
      if (t < 7) {
#pragma unroll
        for (int i = 0; i < 8; ++i) {
          __builtin_amdgcn_sched_group_barrier(0x008, 1, 0);
          __builtin_amdgcn_sched_group_barrier(0x100, 1, 0);
        }
        __builtin_amdgcn_sched_group_barrier(0x008, 4, 0);
      }
      SB0();
      if (t < 4) issue1(t + 4);
      if (t == 4) issue_hh(hu0 + 1, 1, 0);
      if (t == 6) { issue_hh(hu2, 0, 2); issue_hh(hu2, 1, 3); }
      SB0();
      if (r == (t >> 1)) {
#pragma unroll
        for (int k = 0; k < 2; ++k) { wsA[0] = MFMA(ones, Wf[t & 1][k][0], wsA[0]); wsA[1] = MFMA(ones, Wf[t & 1][k][1], wsA[1]); }
      }
      SB0();
    }
  }
  {
    const int j = i16 & 3;
    float d = j == 0 ? sQ[0] : j == 1 ? sQ[1] : j == 2 ? sQ[2] : sQ[3];
    if (q == (i16 >> 2)) { f32x2 v = {sS[0], d}; *(LAS f32x2*)(L + M_STAT + (r * 32 + g * 16 + i16) * 8) = v; }
    if (q == 0) {
      *(LAS float*)(L + M_WS + ((g * 4 + r) * 32 + i16) * 4) = wsA[0][0];
      *(LAS float*)(L + M_WS + ((g * 4 + r) * 32 + 16 + i16) * 4) = wsA[1][0];
    }
  }
  if (wave >= 6) WAIT_V(16);
  WAIT_L0();
  BAR();

  int cvo[4];
#pragma unroll
  for (int ii = 0; ii < 4; ++ii)
    cvo[ii] = ((r * 4 + ii) * 8 + (lane >> 3)) * 4096 + (((lane & 7) ^ (((ii & 1) * 4 + (lane >> 4)) & 7)) << 4) - ii * 1024;
  const int lv = lane * 16;
  auto blkC = [&](int st) -> u32 { return ((0x0541u >> ((st & 3) * 4)) & 15u) * BLK; };
  auto blkAB = [&](int st) -> u32 { return ((0x2876u >> ((st & 3) * 4)) & 15u) * BLK; };
  auto smap = [&](int st) -> int { return ((st & ~3) + ((st + nt2) & 3) + 2 * (stile & 7)) & 15; };
  auto issue_c = [&](int st) { DMA4(c_rs, lb + blkC(st) + r * 4096, cvo[0], cvo[1], cvo[2], cvo[3], smap(st) * 128); };
  const u32 ab_src = WS_AB + (((sg >> 1) * 4 + nt2) * 64 + (sg & 1) * 32) * 8192 + r * 4096;
  auto issue_ab = [&](int st) { DMA4S(ws_r4, lb + blkAB(st) + r * 4096, lv, lv, lv, lv, ab_src + smap(st) * 16384); SB0(); };

  const int c4 = (q & 1) * 2 + (q >> 1);
  float mH[2], rH[2];
  float mA[2], rA[2], mrow[2][4], rrow[2][4], wsv[2];
  auto epiA = [&](int ks) {
    if (ks == 0) {
#pragma unroll
      for (int mt = 0; mt < 2; ++mt) {
        const f32x2 sv = *(const LAS f32x2*)(L + M_STAT + (r * 32 + (mt ^ g) * 16 + i16) * 8);
        const float mean = sv[0] * (1.f / 512.f);
        const float var = fmaxf(sv[1] * (1.f / 512.f) - mean * mean, 0.f);
        const float mb = rbf(mean);
        const float den = rbf(__builtin_amdgcn_sqrtf(rbf(rbf(var) + 1e-5f)));
        const float inv = __builtin_amdgcn_rcpf(den);
        const float m2 = rbf((mean - mb) * inv);
        const float den2 = rbf(__builtin_amdgcn_sqrtf(rbf(rbf(var * inv * inv) + 1e-5f)));
        mA[mt] = mb; rA[mt] = cg < 8 ? inv : -inv;
        rH[mt] = inv * __builtin_amdgcn_rcpf(den2); mH[mt] = (mb + m2 * den) * rH[mt];
      }
    } else if (ks == 1) {
#pragma unroll
      for (int mt = 0; mt < 2; ++mt)
#pragma unroll
        for (int j = 0; j < 4; ++j) { mrow[mt][j] = __shfl(mA[mt], 4 * q + j); rrow[mt][j] = __shfl(rA[mt], 4 * q + j); }
#pragma unroll
      for (int nt = 0; nt < 2; ++nt) {
        wsv[nt] = 0.f;
#pragma unroll
        for (int rr = 0; rr < 4; ++rr) wsv[nt] += *(const LAS float*)(L + M_WS + ((g * 4 + rr) * 32 + nt * 16 + i16) * 4);
      }
    } else if (ks == 3 || ks == 5) {
      const int nt = ks == 5;
      const u32 dst = WS_AB + ((((cg >> 3) * 4 + ((cg & 7) >> 1)) * 64 + stile * 4 + r) * 8 + (cg & 1) * 4 + g * 2) * 1024 + ((c4 ^ (g * 2)) * 16 + i16) * 16;
      u32 p[2][2];
#pragma unroll
      for (int mt = 0; mt < 2; ++mt) {
        float v[4];
#pragma unroll
        for (int j = 0; j < 4; ++j) v[j] = (accA[nt][mt][j] - mrow[mt][j] * wsv[nt]) * rrow[mt][j];
        p[mt][0] = pack2(v[0], v[1]); p[mt][1] = pack2(v[2], v[3]);
      }
      auto s0 = __builtin_amdgcn_permlane16_swap(p[0][0], p[1][0], false, false);
      auto s1 = __builtin_amdgcn_permlane16_swap(p[0][1], p[1][1], false, false);
      u32x4 sv = {s0[0], s1[0], s0[1], s1[1]};
      __builtin_amdgcn_raw_buffer_store_b128(sv, ws_rs, dst + nt * 1024, 0, 16);
    }
  };
  epiA(0); epiA(1); epiA(3); epiA(5);
  SB0();
  auto sideB = [&](int ks) {
    if (ks == 6) {
      WAIT_V(0);
      if (lane == 0)
        __hip_atomic_store((gu64*)(ws + WS_FAB) + (((((cg >> 3) * 4 + ((cg & 7) >> 1)) * 16 + stile) * 2 + (cg & 1)) * 8 + wave), tag, RLX_AGENT);
    } else if (ks == 8 || ks == 10 || ks == 12) {
      if (g == 0) issue_c((ks - 8) >> 1);
    }
  };

  auto h_mma = [&](f32x4 (&acc)[2][2], u32 d0, u32 d1, int slot, auto&& side) {
    const int a = i16 >> 2, p = lane & 3;
    u32 tb[2][2];
#pragma unroll
    for (int ft = 0; ft < 2; ++ft) {
      const u32 o = (8 * q + a) * 64 + ((ft ^ (q & 1)) * 32) + p * 8;
      tb[0][ft] = d0 + o; tb[1][ft] = d1 + o;
      acc[ft][0] = (f32x4){0.f, 0.f, 0.f, 0.f}; acc[ft][1] = (f32x4){0.f, 0.f, 0.f, 0.f};
    }
    {
      f32x4 cs[2] = {};
      const u32 cb = (r < 2 ? d0 : d1) + (r & 1) * 8192 - d0;
#pragma unroll
      for (int k = 0; k < 4; ++k)
#pragma unroll
        for (int ft = 0; ft < 2; ++ft) {
          s16x4 lo = __builtin_amdgcn_ds_read_tr16_b64_v4i16((LAS s16x4*)(L + tb[0][ft] + cb + k * 2048));
          s16x4 hi = __builtin_amdgcn_ds_read_tr16_b64_v4i16((LAS s16x4*)(L + tb[0][ft] + cb + k * 2048 + 256));
          cs[ft] = MFMA(__builtin_shufflevector(lo, hi, 0, 1, 2, 3, 4, 5, 6, 7), ones, cs[ft]);
        }
      if (i16 == 0) {
        *(LAS f32x4*)(L + M_CS + ((slot * 4 + r) * 32 + 4 * q) * 4) = cs[0];
        *(LAS f32x4*)(L + M_CS + ((slot * 4 + r) * 32 + 16 + 4 * q) * 4) = cs[1];
      }
    }
    bf16x8 wf[3][2];
    auto ldw = [&](int ks) {
#pragma unroll
      for (int ft = 0; ft < 2; ++ft) {
        s16x4 lo = __builtin_amdgcn_ds_read_tr16_b64_v4i16((LAS s16x4*)(L + tb[ks >> 3][ft] + (ks & 7) * 2048));
        s16x4 hi = __builtin_amdgcn_ds_read_tr16_b64_v4i16((LAS s16x4*)(L + tb[ks >> 3][ft] + (ks & 7) * 2048 + 256));
        wf[ks % 3][ft] = __builtin_shufflevector(lo, hi, 0, 1, 2, 3, 4, 5, 6, 7);
      }
    };
    ldw(0); ldw(1);
    SB0();
#pragma unroll
    for (int ks = 0; ks < 16; ++ks) {
      if (ks + 2 < 16) ldw(ks + 2);
#pragma unroll
      for (int ft = 0; ft < 2; ++ft) {
        acc[ft][0] = MFMA(wf[ks % 3][ft], xf[0][ks], acc[ft][0]);
        acc[ft][1] = MFMA(wf[ks % 3][ft], xf[1][ks], acc[ft][1]);
      }
      side(ks);
      SB0();
    }
  };
  auto preH = [&](f32x4 (&acc)[2][2], int slot, const u32x2* bv, u32 (&pa)[2][2][2]) {
#pragma unroll
    for (int ft = 0; ft < 2; ++ft) {
      f32x4 ecs = *(const LAS f32x4*)(L + M_CS + ((slot * 4 + 0) * 32 + ft * 16 + 4 * q) * 4);
#pragma unroll
      for (int rr = 1; rr < 4; ++rr) ecs += *(const LAS f32x4*)(L + M_CS + ((slot * 4 + rr) * 32 + ft * 16 + 4 * q) * 4);
      const float bj[4] = {lo_f(bv[ft][0]), hi_f(bv[ft][0]), lo_f(bv[ft][1]), hi_f(bv[ft][1])};
#pragma unroll
      for (int mt = 0; mt < 2; ++mt) {
        float v[4];
#pragma unroll
        for (int jj = 0; jj < 4; ++jj) v[jj] = acc[ft][mt][jj] * rH[mt] + (bj[jj] - mH[mt] * ecs[jj]);
        pa[mt][ft][0] = pack2(v[0], v[1]); pa[mt][ft][1] = pack2(v[2], v[3]);
      }
    }
  };
  auto actH = [&](u32 p00, u32 p01, u32 p10, u32 p11, int unit, int trow16) {
    const u32 dst = WS_H + (((stile * 2 + (r >> 1)) * 43 + unit) * 4 + (r & 1) * 2 + trow16) * 1024 + (c4 * 16 + i16) * 16;
    const u32 k00 = pack2(gelu_t(lo_f(p00)), gelu_t(hi_f(p00))), k01 = pack2(gelu_t(lo_f(p01)), gelu_t(hi_f(p01)));
    const u32 k10 = pack2(gelu_t(lo_f(p10)), gelu_t(hi_f(p10))), k11 = pack2(gelu_t(lo_f(p11)), gelu_t(hi_f(p11)));
    auto s0 = __builtin_amdgcn_permlane16_swap(k00, k10, false, false);
    auto s1 = __builtin_amdgcn_permlane16_swap(k01, k11, false, false);
    u32x4 sv = {s0[0], s1[0], s0[1], s1[1]};
    __builtin_amdgcn_raw_buffer_store_b128(sv, ws_rs, dst, 0, 16);
  };
  auto act2 = [&](u32 p) -> u32 { return pack2(gelu_t(lo_f(p)), gelu_t(hi_f(p))); };
  auto actH_fin = [&](u32 k00, u32 k01, u32 p10, u32 p11, int unit, int trow16) {
    const u32 dst = WS_H + (((stile * 2 + (r >> 1)) * 43 + unit) * 4 + (r & 1) * 2 + trow16) * 1024 + (c4 * 16 + i16) * 16;
    auto s0 = __builtin_amdgcn_permlane16_swap(k00, act2(p10), false, false);
    auto s1 = __builtin_amdgcn_permlane16_swap(k01, act2(p11), false, false);
    u32x4 sv = {s0[0], s1[0], s0[1], s1[1]};
    __builtin_amdgcn_raw_buffer_store_b128(sv, ws_rs, dst, 0, 16);
  };
  f32x4 accB[2][2], accC[2][2];
  h_mma(accB, (6 + 2 * g) * BLK, (g ? 0 : 7) * BLK, g, sideB);
  WAIT_L0();
  BAR();

  const int tile = stile * 4 + nt2;
  if (g == 1) {
    u32 pa[2][2][2];
    preH(accB, 1, bvA, pa);
    poll_flags<2>((gu64*)(ws + WS_FAB) + (((sg >> 1) * 4 + nt2) * 16 + (sg & 1) * 8) * 16 + lane, tag);
    issue_ab(0); issue_ab(1); issue_ab(2); issue_c(3);
    const u32 h_src = WS_H + (stile * 2 + (sg >> 1)) * 43 * 4096;
    const u32x4 w2_rs = mk_rs(w2, 1376 * 512 * 2);
    int wvo[4];
#pragma unroll
    for (int ii = 0; ii < 4; ++ii) {
      const int c = lane & 7, f = ((lane >> 4) & 1) << 1;
      wvo[ii] = (ii * 8 + (lane >> 3)) * 1024 + (nt2 * 128 + (sg & 1) * 64) * 2 + ((((c >> 1) ^ f) << 5) | ((c & 1) << 4)) - ii * 1024;
    }
    const int orot = stile + nt2;
    auto issue_o = [&](int j) {
      int unit;
      if (j < 4) {
        const int c = ((j + orot) & 3) * 4 + r;
        unit = c < 11 ? 3 * c + 1 : 2 * c + 12;
      } else {
        int jj = j - 4 + (orot & 3) + ((orot >> 2) & 1); jj = jj >= 6 ? jj - 6 : jj; jj = j == 10 ? 6 : jj;
        const int t = min(jj * 4 + r, 26);
        unit = t < 22 ? 3 * (t >> 1) + 2 * (t & 1) : 2 * t - 11;
      }
      DMA4S(ws_r4, lb + blkC(j) + r * 4096, lv, lv, lv, lv, h_src + unit * 4096);
      DMA4(w2_rs, lb + blkAB(j) + r * 4096, wvo[0], wvo[1], wvo[2], wvo[3], unit * 32768);
      SB0();
    };
    BAR();
    issue_ab(3);
    actH(pa[0][0][0], pa[0][0][1], pa[0][1][0], pa[0][1][1], hu0 + 1, 1);
    actH(pa[1][0][0], pa[1][0][1], pa[1][1][0], pa[1][1][1], hu0 + 1, 0);
    SB0(); WAIT_V(18); BAR();
    u32x4 pk_;
    u32 ka, kb;
    auto half_a = [&](int u, int mt) {
      pk_ = *(const LAS u32x4*)(L + 3 * BLK + ((u * 4 + r) * 2 + mt) * 1024 + lane * 16);
      ka = act2(pk_[0]); kb = act2(pk_[1]);
      asm volatile("" : "+v"(ka), "+v"(kb));
    };
    auto half_b = [&](int mt, int unit) { actH_fin(ka, kb, pk_[2], pk_[3], unit, mt); };
    SB0(); WAIT_V(14); BAR(); issue_c(4); issue_ab(4);
    half_a(0, 0);
    SB0(); WAIT_V(8); BAR();
    if (lane == 0) __hip_atomic_store((gu64*)(ws + WS_FH) + ((stile * 4 + r) * 16 + cg), tag, RLX_AGENT);
    issue_c(5); issue_ab(5);
    half_b(0, hu0);
    SB0(); WAIT_V(16); BAR(); issue_c(6); issue_ab(6);
    half_a(0, 1);
    SB0(); WAIT_V(16); BAR(); issue_c(7); issue_ab(7);
    half_b(1, hu0);
    SB0(); WAIT_V(16); BAR(); issue_c(8); issue_ab(8);
    if (nh == 3) half_a(1, 0);
    SB0(); WAIT_V(16); BAR(); issue_c(9); issue_ab(9);
    if (nh == 3) half_b(0, hu2);
    SB0(); WAIT_V(16); BAR(); issue_c(10); issue_ab(10);
    if (nh == 3) half_a(1, 1);
    SB0(); WAIT_V(16); BAR(); issue_c(11); issue_ab(11);
    if (nh == 3) half_b(1, hu2);
    SB0(); WAIT_V(16); BAR(); issue_c(12); issue_ab(12);
    {
      const int row = (stile & 7) * 172 + min(r * 64 + lane, 171);
      TOUCH(w2_rs, lb + 3 * BLK + r * 256, row * 1024 + (nt2 * 128 + (sg & 1) * 64) * 2, 0);
    }
    gu64* fhp = (gu64*)(ws + WS_FH) + (stile * 4 + 2 * (sg >> 1)) * 16 + (lane & 31);
    u64 pv;
    SB0(); WAIT_V(8); BAR();
    if (lane == 0) __hip_atomic_store((gu64*)(ws + WS_FH) + 1024 + ((stile * 4 + r) * 16 + cg), tag, RLX_AGENT);
    POLL_ISSUE(pv, fhp);
    issue_c(13); issue_ab(13);
    SB0(); WAIT_V(16); BAR(); issue_c(14); issue_ab(14);
    SB0(); WAIT_V(16); BAR(); issue_c(15); issue_ab(15);
    asm volatile("" : "+v"(pv));
    if (!__all(pv == tag)) poll_flags<1>(fhp, tag);
    SB0(); WAIT_V(16); BAR(); issue_o(0);
    SB0(); WAIT_V(16); BAR(); POLL_ISSUE(pv, fhp + 1024); issue_o(1);
    SB0(); WAIT_V(16); BAR(); issue_o(2);
    SB0(); WAIT_V(16); BAR(); issue_o(3);
    asm volatile("" : "+v"(pv));
    if (!__all(pv == tag)) poll_flags<1>(fhp + 1024, tag);
#pragma unroll 1
    for (int i = 17; i < 21; ++i) { WAIT_V(16); BAR(); issue_o(i - 13); }
    gu64* fpp = (gu64*)(ws + WS_FP) + (tile * 4 + sg) * 8 + (lane & 3);
    gu64* fall = (gu64*)(ws + WS_FAB) + ((bid == 0 && r == 0) ? lane * 8 : 0);
    u64 e0, e1, e2, e3;
    SB0(); WAIT_V(16); BAR();
    POLL_ISSUE(e0, fall); POLL_ISSUE(e1, fall + 512); POLL_ISSUE(e2, fall + 1024); POLL_ISSUE(e3, fall + 1536);
    issue_o(8);
    SB0(); WAIT_V(16); BAR(); POLL_ISSUE(pv, fpp); issue_o(9);
    SB0(); WAIT_V(16); BAR(); issue_o(10);
    SB0(); WAIT_V(16); BAR();
    asm volatile("" : "+v"(pv));
    if (!__all(pv == tag || (lane & 3) == sg)) poll_flags<1>(fpp, tag, (lane & 3) == sg);
    DMA4S(ws_r4, lb + blkC(3) + r * 4096, lv, lv, lv, lv, WS_P + (tile * 4 + sg) * 65536 + (sg ^ (2 + (r >> 1))) * 16384 + (r & 1) * 4096);
    if (r < 2) {
      DMA4S(ws_r4, lb + 3 * BLK + r * 4096, lv, lv, lv, lv, WS_P + (tile * 4 + sg) * 65536 + (sg ^ 1) * 16384 + r * 4096);
    } else {
      int xo[4];
#pragma unroll
      for (int ii = 0; ii < 4; ++ii) xo[ii] = (ii * 8 + (lane >> 3)) * 1024 + (((lane & 7) ^ ((ii * 4 + (lane >> 4)) & 7)) << 4) - ii * 1024;
      DMA4(x_rs, lb + 3 * BLK + r * 4096, xo[0], xo[1], xo[2], xo[3], ((sg >> 1) * 64 + (r - 2) * 32) * 1024 + (nt2 * 128 + (sg & 1) * 64) * 2);
    }
    SB0(); WAIT_V(16); BAR();
    SB0(); WAIT_V(8); BAR();
    SB0(); WAIT_V(0); BAR();
    if (bid == 0 && r == 0) {
      asm volatile("" : "+v"(e0), "+v"(e1), "+v"(e2), "+v"(e3));
      if (!__all(e0 == tag && e1 == tag && e2 == tag && e3 == tag)) {
        for (u32 sp = 0; sp < SPIN_CAP; ++sp) {
          bool ok = true;
#pragma unroll
          for (int k = 0; k < 4; ++k) ok &= __hip_atomic_load(fall + k * 512, RLX_AGENT) == tag;
          if (__all(ok)) break;
          __builtin_amdgcn_s_sleep(1);
        }
      }
      if (lane == 0) __hip_atomic_store((gu64*)(ws + WS_EPOCH), tag, RLX_AGENT);
    }
    BAR();
  } else {
    u32 paB[2][2][2];
    preH(accB, 0, bvA, paB);
    if (nh == 3) h_mma(accC, 2 * BLK, 3 * BLK, 2, [&](int) {});
    WAIT_L0();
    BAR();
#pragma unroll
    for (int mt = 0; mt < 2; ++mt) {
      u32x4 v = {paB[mt][0][0], paB[mt][0][1], paB[mt][1][0], paB[mt][1][1]};
      *(LAS u32x4*)(L + 3 * BLK + (r * 2 + mt) * 1024 + lane * 16) = v;
    }
    if (nh == 3) {
      u32 paC[2][2][2];
      preH(accC, 2, bvC, paC);
#pragma unroll
      for (int mt = 0; mt < 2; ++mt) {
        u32x4 v = {paC[mt][0][0], paC[mt][0][1], paC[mt][1][0], paC[mt][1][1]};
        *(LAS u32x4*)(L + 3 * BLK + ((4 + r) * 2 + mt) * 1024 + lane * 16) = v;
      }
    }
    const int th = r >> 1, eh = r & 1;
    const int l31 = lane & 31, hh = lane >> 5;
    f32x16 acc[2][2] = {};
    {
      const u32 a_off = eh * 4096 + ((lane >> 4) & 1) * 1024 + hh * 256 + i16 * 16;
      u32 c_off[4];
#pragma unroll
      for (int k4 = 0; k4 < 4; ++k4) c_off[k4] = (th * 64 + l31) * 128 + (((k4 * 2 + hh) ^ ((l31 >> 1) & 7)) << 4);
      bf16x8 A0[2][2], B0[2][2], A1[2][2], B1[2][2];
      auto rd = [&](bf16x8 (*A)[2], bf16x8 (*B)[2], int st, int kh) {
        LAS char* sa = L + blkAB(st) + a_off;
        LAS char* sc = L + blkC(st);
#pragma unroll
        for (int k2 = 0; k2 < 2; ++k2)
#pragma unroll
          for (int e = 0; e < 2; ++e) {
            A[k2][e] = *(const LAS bf16x8*)(sa + kh * 8192 + e * 2048 + k2 * 512);
            B[k2][e] = *(const LAS bf16x8*)(sc + c_off[kh * 2 + k2] + e * 4096);
          }
      };
      auto mm = [&](bf16x8 (*A)[2], bf16x8 (*B)[2]) {
#pragma unroll
        for (int k2 = 0; k2 < 2; ++k2)
#pragma unroll
          for (int e = 0; e < 2; ++e)
#pragma unroll
            for (int t = 0; t < 2; ++t) acc[e][t] = MFMA32(A[k2][e], B[k2][t], acc[e][t]);
      };
      auto sched = [&]() {
#pragma unroll
        for (int i = 0; i < 4; ++i) {
          __builtin_amdgcn_sched_group_barrier(0x008, 1, 0);
          __builtin_amdgcn_sched_group_barrier(0x100, 2, 0);
        }
        __builtin_amdgcn_sched_group_barrier(0x008, 4, 0);
      };
      __builtin_amdgcn_s_setprio(2);
      WAIT_V(0); WAIT_L0(); BAR();
      rd(A0, B0, 0, 0);
#pragma unroll 1
      for (int st = 0; st < 15; ++st) {
        rd(A1, B1, st, 1);
        mm(A0, B0);
        sched();
        WAIT_L0(); BAR();
        rd(A0, B0, st + 1, 0);
        mm(A1, B1);
        sched();
        SB0();
      }
      rd(A1, B1, 15, 1);
      mm(A0, B0);
      sched();
      WAIT_L0(); BAR();
      mm(A1, B1);
    }
    if (r == sg) {
#pragma unroll
      for (int e = 0; e < 2; ++e)
#pragma unroll
        for (int t = 0; t < 2; ++t)
#pragma unroll
          for (int rg = 0; rg < 4; ++rg) {
            f32x4 v = {acc[e][t][4 * rg], acc[e][t][4 * rg + 1], acc[e][t][4 * rg + 2], acc[e][t][4 * rg + 3]};
            *(LAS f32x4*)(L + MISC + ((e * 2 + t) * 4 + rg) * 1024 + lane * 16) = v;
          }
    }
    const u32 pdst = WS_P + ((tile * 4 + r) * 4 + sg) * 16384 + lane * 16;
    auto pstore = [&](int i) {
      if (r != sg) {
        {
          const int e = i >> 2, t = (i >> 1) & 1, h2 = i & 1;
          u32x4 v = {pack2(acc[e][t][8 * h2], acc[e][t][8 * h2 + 1]), pack2(acc[e][t][8 * h2 + 2], acc[e][t][8 * h2 + 3]),
                     pack2(acc[e][t][8 * h2 + 4], acc[e][t][8 * h2 + 5]), pack2(acc[e][t][8 * h2 + 6], acc[e][t][8 * h2 + 7])};
          __builtin_amdgcn_raw_buffer_store_b128(v, ws_rs, pdst + i * 1024, 0, 16);
        }
      }
    };
    const int dh = r & 1, t2 = r >> 1;
    const int d0 = nt2 * 128 + (sg & 1) * 64 + dh * 32 + 4 * hh;
    f32x16 oc = {};
    u32x2 bv[4];
    {
      const int a = i16 >> 2, p = lane & 3;
      const u32 wb = (8 * hh + a) * 128 + (((dh * 2 + ((lane >> 4) & 1)) ^ (a & 2)) * 32) + p * 8;
      const u32 hb = t2 * 2048 + ((lane >> 4) & 1) * 1024 + hh * 256 + i16 * 16;
      bf16x8 A0[2], B0[2], A1[2], B1[2];
      auto rd = [&](bf16x8* A, bf16x8* B, int j, int kk) {
        LAS char* sw2 = L + blkAB(j) + wb;
        LAS char* sh = L + blkC(j) + hb;
#pragma unroll
        for (int k2 = 0; k2 < 2; ++k2) {
          s16x4 lo = __builtin_amdgcn_ds_read_tr16_b64_v4i16((LAS s16x4*)(sw2 + kk * 4096 + k2 * 2048));
          s16x4 hi = __builtin_amdgcn_ds_read_tr16_b64_v4i16((LAS s16x4*)(sw2 + kk * 4096 + k2 * 2048 + 512));
          A[k2] = __builtin_shufflevector(lo, hi, 0, 1, 2, 3, 4, 5, 6, 7);
          B[k2] = *(const LAS bf16x8*)(sh + kk * 4096 + k2 * 512);
        }
      };
      auto mm = [&](bf16x8* A, bf16x8* B) {
        oc = MFMA32(A[0], B[0], oc);
        oc = MFMA32(A[1], B[1], oc);
      };
      auto sched = [&]() {
        __builtin_amdgcn_sched_group_barrier(0x100, 3, 0);
        __builtin_amdgcn_sched_group_barrier(0x008, 1, 0);
        __builtin_amdgcn_sched_group_barrier(0x100, 3, 0);
        __builtin_amdgcn_sched_group_barrier(0x008, 1, 0);
        SB0();
      };
      rd(A0, B0, 0, 0);
      SB0();
#pragma unroll
      for (int j = 0; j < 2; ++j) {
        rd(A1, B1, j, 1); mm(A0, B0); sched(); pstore(4 * j);
        rd(A0, B0, j, 2); mm(A1, B1); sched(); pstore(4 * j + 1);
        rd(A1, B1, j, 3); mm(A0, B0); sched(); pstore(4 * j + 2);
        WAIT_L0(); BAR();
        rd(A0, B0, j + 1, 0); mm(A1, B1); sched(); pstore(4 * j + 3);
      }
#pragma unroll
      for (int rg = 0; rg < 4; ++rg) {
        asm volatile("global_load_dwordx2 %0, %1, off" : "=v"(bv[rg]) : "v"(b2 + d0 + rg * 8) : "memory");
      }
#pragma unroll 1
      for (int j = 2; j < 10; ++j) {
        rd(A1, B1, j, 1); mm(A0, B0); sched();
        rd(A0, B0, j, 2); mm(A1, B1); sched();
        rd(A1, B1, j, 3); mm(A0, B0); sched();
        if (j == 3) {
          WAIT_V(0);
          if (lane == 0 && r != sg) __hip_atomic_store((gu64*)(ws + WS_FP) + (tile * 4 + r) * 8 + sg, tag, RLX_AGENT);
        }
        WAIT_L0(); BAR();
        rd(A0, B0, j + 1, 0); mm(A1, B1); sched();
      }
      rd(A1, B1, 10, 1); mm(A0, B0); sched();
      rd(A0, B0, 10, 2); mm(A1, B1); sched();
      mm(A0, B0);
    }
    WAIT_V(0);
#pragma unroll
    for (int rg = 0; rg < 4; ++rg) asm volatile("" : "+v"(bv[rg]));
    float gate[16];
#pragma unroll
    for (int rg = 0; rg < 4; ++rg) {
      const float bb[4] = {lo_f(bv[rg][0]), hi_f(bv[rg][0]), lo_f(bv[rg][1]), hi_f(bv[rg][1])};
#pragma unroll
      for (int j = 0; j < 4; ++j) {
        const float o2 = oc[4 * rg + j] + bb[j];
        gate[4 * rg + j] = o2 * sigm(o2);
      }
    }
#pragma unroll
    for (int j = 0; j < 16; j += 4) asm volatile("" : "+v"(gate[j]), "+v"(gate[j + 1]), "+v"(gate[j + 2]), "+v"(gate[j + 3]));
    BAR();
#pragma unroll
    for (int rg = 0; rg < 4; ++rg) {
      const u32 o = ((dh * 2 + t2) * 4 + rg) * 1024 + lane * 16;
      const f32x4 y0 = *(const LAS f32x4*)(L + MISC + o);
      const u32 ob = ((dh * 2 + t2) * 2 + (rg >> 1)) * 1024 + lane * 16 + (rg & 1) * 8;
      const u32x2 q1 = *(const LAS u32x2*)(L + 3 * BLK + ob), q2 = *(const LAS u32x2*)(L + 0 * BLK + ob), q3 = *(const LAS u32x2*)(L + 0 * BLK + 8192 + ob);
      LAS u32x2* xp = (LAS u32x2*)(L + 3 * BLK + 8192 + (t2 * 32 + l31) * 128 + (((dh * 4 + rg) ^ ((l31 >> 1) & 7)) << 4) + hh * 8);
      const u32x2 xv = *xp;
      const float ya[4] = {y0[0] + lo_f(q1[0]), y0[1] + hi_f(q1[0]), y0[2] + lo_f(q1[1]), y0[3] + hi_f(q1[1])};
      const float yb[4] = {lo_f(q2[0]) + lo_f(q3[0]), hi_f(q2[0]) + hi_f(q3[0]), lo_f(q2[1]) + lo_f(q3[1]), hi_f(q2[1]) + hi_f(q3[1])};
      const float xx[4] = {lo_f(xv[0]), hi_f(xv[0]), lo_f(xv[1]), hi_f(xv[1])};
      float res[4];
#pragma unroll
      for (int j = 0; j < 4; ++j) res[j] = xx[j] + (ya[j] + yb[j]) * gate[4 * rg + j];
      u32x2 ov = {pack2(res[0], res[1]), pack2(res[2], res[3])};
      *xp = ov;
    }
    WAIT_L0();
    BAR();
  }
  {
    const int row = tid >> 3, ch = tid & 7;
    const u32x4 v = *(const LAS u32x4*)(L + 3 * BLK + 8192 + row * 128 + ch * 16);
    *(u32x4*)(out + (size_t)(stile * 128 + (sg >> 1) * 64 + row) * 512 + nt2 * 128 + (sg & 1) * 64 + ((ch ^ ((row >> 1) & 7)) << 3)) = v;
  }
}

extern "C" void kernel_launch(void* const* d_in, const int* in_sizes, int n_in, void* d_out, int out_size, void* d_ws, size_t ws_size,
                              hipStream_t stream) {
  fused_k<<<256, 512, 0, stream>>>((const u16*)d_in[0], (const u16*)d_in[1], (const u16*)d_in[2], (const u16*)d_in[3], (const u16*)d_in[4],
                                   (const u16*)d_in[6], (const u16*)d_in[7], (const u16*)d_in[8], (const u16*)d_in[9], (u16*)d_out,
                                   (char*)d_ws);
}
```

```cpp
#include <hip/hip_runtime.h>
#include <hip/hip_bf16.h>
#include <stdint.h>

typedef unsigned short u16;
typedef unsigned int u32;
typedef unsigned long long u64;
typedef __attribute__((ext_vector_type(8))) short bf16x8;
typedef __attribute__((ext_vector_type(4))) short s16x4;
typedef __attribute__((ext_vector_type(2))) __bf16 bf2;
typedef __attribute__((ext_vector_type(4))) float f32x4;
typedef __attribute__((ext_vector_type(2))) float f32x2;
typedef __attribute__((ext_vector_type(16))) float f32x16;
typedef __attribute__((ext_vector_type(4))) unsigned u32x4;
typedef __attribute__((ext_vector_type(2))) unsigned u32x2;
#define LAS __attribute__((address_space(3)))
#define GAS __attribute__((address_space(1)))
typedef GAS u64 gu64;

constexpr u32 WS_EPOCH = 0;
constexpr u32 WS_FAB = 4096;
constexpr u32 WS_FH = WS_FAB + 16384;
constexpr u32 WS_FP = WS_FH + 16384;
constexpr u32 WS_AB = 1u << 20;
constexpr u32 WS_H = WS_AB + (4u << 20);
constexpr u32 WS_P = WS_H + (6u << 20);
constexpr u32 WS_END = WS_P + (16u << 20);

constexpr u32 BLK = 16384;
constexpr int CODE_LINES = 240;
constexpr u32 MISC = 9 * BLK;
constexpr u32 M_STAT = MISC;
constexpr u32 M_WS = MISC + 1024;
constexpr u32 M_CS = MISC + 2048;

#define RLX_AGENT __ATOMIC_RELAXED, __HIP_MEMORY_SCOPE_AGENT
#define WAIT_V(n) asm volatile("s_waitcnt vmcnt(" #n ")" ::: "memory")
#define WAIT_L0() asm volatile("s_waitcnt lgkmcnt(0)" ::: "memory")
#define BAR() __builtin_amdgcn_s_barrier()
#define SB0() __builtin_amdgcn_sched_barrier(0)
#define RFL(x) ((u32)__builtin_amdgcn_readfirstlane((u32)(x)))
constexpr u32 SPIN_CAP = 1u << 18;


__device__ __forceinline__ float rbf(float v) { return (float)(__bf16)v; }
__device__ __forceinline__ u32 pack2(float a, float b) { bf2 v = {(__bf16)a, (__bf16)b}; return __builtin_bit_cast(u32, v); }
__device__ __forceinline__ float lo_f(u32 v) { return __builtin_bit_cast(float, v << 16); }
__device__ __forceinline__ float hi_f(u32 v) { return __builtin_bit_cast(float, v & 0xffff0000u); }
__device__ __forceinline__ float sigm(float z) {
  return __builtin_amdgcn_rcpf(1.0f + __builtin_amdgcn_exp2f(-1.4426950408889634f * z));
}
__device__ __forceinline__ float gelu_t(float u) {
  const float z = u * (-2.3022081983f - 0.1029432398f * u * u);
  return u * __builtin_amdgcn_rcpf(1.0f + __builtin_amdgcn_exp2f(z));
}
__device__ __forceinline__ u32x4 mk_rs(const void* p, u32 bytes) {
  const u64 a = (u64)p;
  u32x4 r = {RFL(a), RFL((u32)(a >> 32) & 0xffffu), bytes, 0x00020000u};
  return r;
}
#define DMA4M(MOD, rs, d, v0, v1, v2, v3, so) do { u32 keep_; \
  asm volatile("s_nop 4\n\ts_mov_b32 %0, m0\n\ts_mov_b32 m0, %1\n\ts_nop 0\n\t" \
               "buffer_load_dwordx4 %2, %6, %7 offen " MOD " lds\n\t" \
               "buffer_load_dwordx4 %3, %6, %7 offen offset:1024 " MOD " lds\n\t" \
               "buffer_load_dwordx4 %4, %6, %7 offen offset:2048 " MOD " lds\n\t" \
               "buffer_load_dwordx4 %5, %6, %7 offen offset:3072 " MOD " lds\n\t" \
               "s_mov_b32 m0, %0" \
               : "=&s"(keep_) : "s"(RFL(d)), "v"(v0), "v"(v1), "v"(v2), "v"(v3), "s"(rs), "s"(RFL(so)) : "memory"); } while (0)
#define DMA4(rs, d, v0, v1, v2, v3, so) DMA4M("", rs, d, v0, v1, v2, v3, so)
#define DMA4S(rs, d, v0, v1, v2, v3, so) DMA4M("sc1", rs, d, v0, v1, v2, v3, so)
#define TOUCH(rs, d, v0, so) do { u32 keep_; \
  asm volatile("s_nop 4\n\ts_mov_b32 %0, m0\n\ts_mov_b32 m0, %1\n\ts_nop 0\n\t" \
               "buffer_load_dword %2, %3, %4 offen lds\n\t" \
               "s_mov_b32 m0, %0" \
               : "=&s"(keep_) : "s"(RFL(d)), "v"(v0), "s"(rs), "s"(RFL(so)) : "memory"); } while (0)
#define TOUCHG(d, p) do { u32 keep_; \
  asm volatile("s_mov_b32 %0, m0\n\ts_mov_b32 m0, %1\n\ts_nop 0\n\t" \
               "global_load_lds_dword %2, off\n\t" \
               "s_mov_b32 m0, %0" \
               : "=&s"(keep_) : "s"(RFL(d)), "v"(p) : "memory"); } while (0)
#define MFMA(a, b, c) __builtin_amdgcn_mfma_f32_16x16x32_bf16(a, b, c, 0, 0, 0)
#define MFMA32(a, b, c) __builtin_amdgcn_mfma_f32_32x32x16_bf16(a, b, c, 0, 0, 0)

template <bool B> struct BoolC { static constexpr bool value = B; };

template <int NPER>
__device__ __forceinline__ void poll_flags(gu64* f, u64 tag, bool skip = false) {
  for (u32 sp = 0; sp < SPIN_CAP; ++sp) {
    bool ok = true;
#pragma unroll
    for (int k = 0; k < NPER; ++k) {
      u64 v = __hip_atomic_load(f + k * 64, RLX_AGENT);
      ok &= (v == tag);
    }
    if (__all(ok || skip)) break;
    __builtin_amdgcn_s_sleep(1);
  }
  asm volatile("" ::: "memory");
}
#define POLL_ISSUE(val, ptr) asm volatile("global_load_dwordx2 %0, %1, off sc1" : "=v"(val) : "v"(ptr) : "memory")

__global__ __launch_bounds__(512, 2) void fused_k(const u16* __restrict__ x, const u16* __restrict__ Cr, const u16* __restrict__ Ci,
                                                   const u16* __restrict__ Wr, const u16* __restrict__ Wi, const u16* __restrict__ w1,
                                                   const u16* __restrict__ b1, const u16* __restrict__ w2, const u16* __restrict__ b2,
                                                   u16* __restrict__ out, char* __restrict__ ws) {
  __shared__ __attribute__((aligned(1024))) char smem[163840];
  const int tid = threadIdx.x, lane = tid & 63;
  const int wave = __builtin_amdgcn_readfirstlane(tid >> 6);
  const int bid = blockIdx.x, xcd = bid & 7, idx = bid >> 3;
  const int stile = (xcd & 1) * 8 + (idx & 7);
  const int sg = xcd >> 1;
  const int nt2 = idx >> 3;
  const int cg = sg * 4 + nt2;
  const int g = wave >> 2, r = wave & 3;
  const int i16 = lane & 15, q = lane >> 4;
  LAS char* L = (LAS char*)smem;
  const u32 lb = (u32)(size_t)L;

  {
    const u64 pc0 = (u64)__builtin_amdgcn_s_getpc() & ~255ull;
    const int ln = idx * 8 + (lane & 7);
    if (wave == 7) TOUCHG(lb + MISC + 4096, (const char*)pc0 + (ln < CODE_LINES ? ln : 0) * 128);
  }
  const __amdgpu_buffer_rsrc_t ws_rs = __builtin_amdgcn_make_buffer_rsrc((void*)ws, 0, WS_END, 0x00020000);
  const u32x4 ws_r4 = mk_rs(ws, WS_END);
  const u32x4 c_rs = mk_rs((sg < 2 ? Cr : Ci) + (size_t)stile * 128 * 2048 + (sg & 1) * 1024, 128 * 4096);

  const int nh = cg < 11 ? 3 : 2;
  const int hu0 = cg < 11 ? 3 * cg : 33 + 2 * (cg - 11);
  const int hu2 = cg < 11 ? hu0 + 2 : hu0;
  u32x2 bvA[2], bvC[2];
#pragma unroll
  for (int ft = 0; ft < 2; ++ft) {
    asm volatile("global_load_dwordx2 %0, %1, off" : "=v"(bvA[ft]) : "v"(b1 + (hu0 + g) * 32 + ft * 16 + 4 * q) : "memory");
    asm volatile("global_load_dwordx2 %0, %1, off" : "=v"(bvC[ft]) : "v"(b1 + hu2 * 32 + ft * 16 + 4 * q) : "memory");
  }
  u64 tag;
  POLL_ISSUE(tag, (gu64*)(ws + WS_EPOCH));

  const u32x4 x_rs = mk_rs(x + (size_t)stile * 128 * 512, 131072);
  constexpr u32 SLOT = 24576;
  const u32x4 d_rs = wave < 4 ? x_rs : mk_rs((cg < 8 ? Wr : Wi) + (size_t)(cg & 7) * 64 * 512, 65536);
  int dv[4];
#pragma unroll
  for (int ii = 0; ii < 4; ++ii) dv[ii] = (ii * 8 + (lane >> 3)) * 1024 + (((lane & 7) ^ ((ii * 4 + (lane >> 4)) & 7)) << 4) - ii * 1024;
  auto issue1 = [&](int t) {
    if (wave < 6) DMA4(d_rs, lb + wave * 4096 + (t & 3) * SLOT, dv[0], dv[1], dv[2], dv[3], (wave & 3) * 32768 + t * 128);
  };
  const u32x4 w1_rs = mk_rs(w1, 512 * 1376 * 2);
  constexpr int W1ST = 16 * 2752 - 1024;
  const int w1vo = (lane >> 2) * 2752 + (((((lane & 3) >> 1) ^ (lane >> 5)) & 1) << 5) + ((lane & 1) << 4);
  auto issue_hh = [&](int unit, int half, u32 blk) {
    if (wave >= 6) {
      const int p = half * 4 + (wave - 6) * 2;
      DMA4(w1_rs, lb + blk * BLK + (wave - 6) * 8192, w1vo, w1vo + W1ST, w1vo + 2 * W1ST, w1vo + 3 * W1ST, p * 64 * 2752 + unit * 64);
      DMA4(w1_rs, lb + blk * BLK + (wave - 6) * 8192 + 4096, w1vo, w1vo + W1ST, w1vo + 2 * W1ST, w1vo + 3 * W1ST, (p + 1) * 64 * 2752 + unit * 64);
    }
  };
  int w2v[4];
#pragma unroll
  for (int ii = 0; ii < 4; ++ii) {
    const int c = lane & 7, f = ((lane >> 4) & 1) | ((ii & 1) << 1);
    w2v[ii] = (ii * 8 + (lane >> 3)) * 2752 + ((((c >> 1) ^ f) << 5) | ((c & 1) << 4)) - ii * 1024;
  }
  auto issue_hw = [&](int qk, u32 blk) {
    if (wave >= 6) {
      const int r0 = qk * 128 + (wave - 6) * 64;
      DMA4(w1_rs, lb + blk * BLK + (wave - 6) * 8192, w2v[0], w2v[1], w2v[2], w2v[3], r0 * 2752 + hu0 * 64);
      DMA4(w1_rs, lb + blk * BLK + (wave - 6) * 8192 + 4096, w2v[0], w2v[1], w2v[2], w2v[3], (r0 + 32) * 2752 + hu0 * 64);
    }
  };
  issue1(0); BAR(); issue1(1); BAR(); issue1(2); issue1(3);
  if (g == 1) __builtin_amdgcn_s_setprio(1);

  const bf16x8 ones = {0x3f80, 0x3f80, 0x3f80, 0x3f80, 0x3f80, 0x3f80, 0x3f80, 0x3f80};
  bf16x8 xf[2][16];
  f32x4 accA[2][2] = {};
  f32x4 sS = {}, sQ = {}, wsA[2] = {};
  {
    u32 xo[2][2], wo[2];
#pragma unroll
    for (int k = 0; k < 2; ++k) {
      const u32 sw = i16 * 128 + (((k * 4 + q) ^ (i16 >> 1)) << 4);
      xo[k][0] = r * 4096 + g * 2048 + sw; xo[k][1] = r * 4096 + (g ^ 1) * 2048 + sw;
      wo[k] = 16384 + g * 4096 + sw;
    }
    bf16x8 Wf[2][2][2];
    auto rd = [&](int t) {
      const u32 sb = (t & 3) * SLOT;
#pragma unroll
      for (int k = 0; k < 2; ++k) {
        xf[0][2 * t + k] = *(const LAS bf16x8*)(L + sb + xo[k][0]); xf[1][2 * t + k] = *(const LAS bf16x8*)(L + sb + xo[k][1]);
        Wf[t & 1][k][0] = *(const LAS bf16x8*)(L + sb + wo[k]); Wf[t & 1][k][1] = *(const LAS bf16x8*)(L + sb + wo[k] + 2048);
      }
    };
    if (wave < 6) WAIT_V(12); else WAIT_V(0);
    asm volatile("" : "+v"(bvA[0]), "+v"(bvA[1]), "+v"(bvC[0]), "+v"(bvC[1]), "+v"(tag));
    tag += 1ull;
    BAR();
    rd(0);
    SB0();
#pragma unroll
    for (int t = 0; t < 8; ++t) {
      if (t < 7) {
        if (wave < 6) { if (t < 5) WAIT_V(8); else if (t == 5) WAIT_V(4); else WAIT_V(0); }
        WAIT_L0();
        BAR();
        rd(t + 1);
      }
#pragma unroll
      for (int k = 0; k < 2; ++k) {
        const int ks = 2 * t + k;
#pragma unroll
        for (int nt = 0; nt < 2; ++nt) {
          accA[nt][0] = MFMA(xf[0][ks], Wf[t & 1][k][nt], accA[nt][0]);
          accA[nt][1] = MFMA(xf[1][ks], Wf[t & 1][k][nt], accA[nt][1]);
        }
        sS = MFMA(ones, xf[0][ks], sS);
        sQ = MFMA(xf[0][ks], xf[0][ks], sQ);
      }
      if (t < 7) {
#pragma unroll
        for (int i = 0; i < 8; ++i) {
          __builtin_amdgcn_sched_group_barrier(0x008, 1, 0);
          __builtin_amdgcn_sched_group_barrier(0x100, 1, 0);
        }
        __builtin_amdgcn_sched_group_barrier(0x008, 4, 0);
      }
      SB0();
      if (t < 4) issue1(t + 4);
      if (t < 3) issue_hw(t, 6 + t);
      if (t == 4) issue_hw(3, 0);
      if (t == 6) { issue_hh(hu2, 0, 2); issue_hh(hu2, 1, 3); }
      SB0();
      if (r == (t >> 1)) {
#pragma unroll
        for (int k = 0; k < 2; ++k) { wsA[0] = MFMA(ones, Wf[t & 1][k][0], wsA[0]); wsA[1] = MFMA(ones, Wf[t & 1][k][1], wsA[1]); }
      }
      SB0();
    }
  }
  {
    const int j = i16 & 3;
    float d = j == 0 ? sQ[0] : j == 1 ? sQ[1] : j == 2 ? sQ[2] : sQ[3];
    if (q == (i16 >> 2)) { f32x2 v = {sS[0], d}; *(LAS f32x2*)(L + M_STAT + (r * 32 + g * 16 + i16) * 8) = v; }
    if (q == 0) {
      *(LAS float*)(L + M_WS + ((g * 4 + r) * 32 + i16) * 4) = wsA[0][0];
      *(LAS float*)(L + M_WS + ((g * 4 + r) * 32 + 16 + i16) * 4) = wsA[1][0];
    }
  }
  if (wave >= 6) WAIT_V(16);
  WAIT_L0();
  BAR();

  int cvo[4];
#pragma unroll
  for (int ii = 0; ii < 4; ++ii)
    cvo[ii] = ((r * 4 + ii) * 8 + (lane >> 3)) * 4096 + (((lane & 7) ^ (((ii & 1) * 4 + (lane >> 4)) & 7)) << 4) - ii * 1024;
  const int lv = lane * 16;
  auto blkC = [&](int st) -> u32 { return ((0x0541u >> ((st & 3) * 4)) & 15u) * BLK; };
  auto blkAB = [&](int st) -> u32 { return ((0x2876u >> ((st & 3) * 4)) & 15u) * BLK; };
  auto smap = [&](int st) -> int { return ((st & ~3) + ((st + nt2) & 3) + 2 * (stile & 7)) & 15; };
  auto issue_c = [&](int st) { DMA4(c_rs, lb + blkC(st) + r * 4096, cvo[0], cvo[1], cvo[2], cvo[3], smap(st) * 128); };
  const u32 ab_src = WS_AB + (((sg >> 1) * 4 + nt2) * 64 + (sg & 1) * 32) * 8192 + r * 4096;
  auto issue_ab = [&](int st) { DMA4S(ws_r4, lb + blkAB(st) + r * 4096, lv, lv, lv, lv, ab_src + smap(st) * 16384); SB0(); };

  const int c4 = (q & 1) * 2 + (q >> 1);
  float mH[2], rH[2];
  float mA[2], rA[2], mrow[2][4], rrow[2][4], wsv[2];
  auto epiA = [&](int ks) {
    if (ks == 0) {
#pragma unroll
      for (int mt = 0; mt < 2; ++mt) {
        const f32x2 sv = *(const LAS f32x2*)(L + M_STAT + (r * 32 + (mt ^ g) * 16 + i16) * 8);
        const float mean = sv[0] * (1.f / 512.f);
        const float var = fmaxf(sv[1] * (1.f / 512.f) - mean * mean, 0.f);
        const float mb = rbf(mean);
        const float den = rbf(__builtin_amdgcn_sqrtf(rbf(rbf(var) + 1e-5f)));
        const float inv = __builtin_amdgcn_rcpf(den);
        const float m2 = rbf((mean - mb) * inv);
        const float den2 = rbf(__builtin_amdgcn_sqrtf(rbf(rbf(var * inv * inv) + 1e-5f)));
        mA[mt] = mb; rA[mt] = cg < 8 ? inv : -inv;
        rH[mt] = inv * __builtin_amdgcn_rcpf(den2); mH[mt] = (mb + m2 * den) * rH[mt];
      }
    } else if (ks == 1) {
#pragma unroll
      for (int mt = 0; mt < 2; ++mt)
#pragma unroll
        for (int j = 0; j < 4; ++j) { mrow[mt][j] = __shfl(mA[mt], 4 * q + j); rrow[mt][j] = __shfl(rA[mt], 4 * q + j); }
#pragma unroll
      for (int nt = 0; nt < 2; ++nt) {
        wsv[nt] = 0.f;
#pragma unroll
        for (int rr = 0; rr < 4; ++rr) wsv[nt] += *(const LAS float*)(L + M_WS + ((g * 4 + rr) * 32 + nt * 16 + i16) * 4);
      }
    } else if (ks == 3 || ks == 5) {
      const int nt = ks == 5;
      const u32 dst = WS_AB + ((((cg >> 3) * 4 + ((cg & 7) >> 1)) * 64 + stile * 4 + r) * 8 + (cg & 1) * 4 + g * 2) * 1024 + ((c4 ^ (g * 2)) * 16 + i16) * 16;
      u32 p[2][2];
#pragma unroll
      for (int mt = 0; mt < 2; ++mt) {
        float v[4];
#pragma unroll
        for (int j = 0; j < 4; ++j) v[j] = (accA[nt][mt][j] - mrow[mt][j] * wsv[nt]) * rrow[mt][j];
        p[mt][0] = pack2(v[0], v[1]); p[mt][1] = pack2(v[2], v[3]);
      }
      auto s0 = __builtin_amdgcn_permlane16_swap(p[0][0], p[1][0], false, false);
      auto s1 = __builtin_amdgcn_permlane16_swap(p[0][1], p[1][1], false, false);
      u32x4 sv = {s0[0], s1[0], s0[1], s1[1]};
      __builtin_amdgcn_raw_buffer_store_b128(sv, ws_rs, dst + nt * 1024, 0, 16);
    }
  };
  epiA(0); epiA(1); epiA(3); epiA(5);
  SB0();
  auto sideB = [&](int ks) {
    constexpr int KF = 6;
    if (ks == KF) {
      WAIT_V(0);
      if (lane == 0)
        __hip_atomic_store((gu64*)(ws + WS_FAB) + (((((cg >> 3) * 4 + ((cg & 7) >> 1)) * 16 + stile) * 2 + (cg & 1)) * 8 + wave), tag, RLX_AGENT);
    } else if (ks == 8 || ks == 10 || ks == 12) {
      if (g == 0) issue_c((ks - 8) >> 1);
    }
  };

  auto h_mma = [&](f32x4 (&acc)[2][2], auto wide_c, u32 d0, u32 d1, int slot, auto&& side) {
    constexpr bool WIDE = decltype(wide_c)::value;
    constexpr int KSB = WIDE ? 4096 : 2048, HI = WIDE ? 512 : 256, SPLIT = WIDE ? 12 : 8;
    const int a = i16 >> 2, p = lane & 3;
    u32 tb[2][2];
#pragma unroll
    for (int ft = 0; ft < 2; ++ft) {
      const u32 o = WIDE ? (8 * q + a) * 128 + (((g * 2 + ft) ^ ((a >> 1) | ((q & 1) << 1))) * 32) + p * 8
                         : (8 * q + a) * 64 + ((ft ^ (q & 1)) * 32) + p * 8;
      tb[0][ft] = d0 + o; tb[1][ft] = d1 + o;
      acc[ft][0] = (f32x4){0.f, 0.f, 0.f, 0.f}; acc[ft][1] = (f32x4){0.f, 0.f, 0.f, 0.f};
    }
    {
      f32x4 cs[2] = {};
      const u32 cb = WIDE ? (r < 3 ? (u32)r * 16384 : d1 - d0) : (r < 2 ? d0 : d1) + (r & 1) * 8192 - d0;
#pragma unroll
      for (int k = 0; k < 4; ++k)
#pragma unroll
        for (int ft = 0; ft < 2; ++ft) {
          s16x4 lo = __builtin_amdgcn_ds_read_tr16_b64_v4i16((LAS s16x4*)(L + tb[0][ft] + cb + k * KSB));
          s16x4 hi = __builtin_amdgcn_ds_read_tr16_b64_v4i16((LAS s16x4*)(L + tb[0][ft] + cb + k * KSB + HI));
          cs[ft] = MFMA(__builtin_shufflevector(lo, hi, 0, 1, 2, 3, 4, 5, 6, 7), ones, cs[ft]);
        }
      if (i16 == 0) {
        *(LAS f32x4*)(L + M_CS + ((slot * 4 + r) * 32 + 4 * q) * 4) = cs[0];
        *(LAS f32x4*)(L + M_CS + ((slot * 4 + r) * 32 + 16 + 4 * q) * 4) = cs[1];
      }
    }
    bf16x8 wf[3][2];
    auto ldw = [&](int ks) {
      const int hb = ks >= SPLIT, ko = (ks - hb * SPLIT) * KSB;
#pragma unroll
      for (int ft = 0; ft < 2; ++ft) {
        s16x4 lo = __builtin_amdgcn_ds_read_tr16_b64_v4i16((LAS s16x4*)(L + tb[hb][ft] + ko));
        s16x4 hi = __builtin_amdgcn_ds_read_tr16_b64_v4i16((LAS s16x4*)(L + tb[hb][ft] + ko + HI));
        wf[ks % 3][ft] = __builtin_shufflevector(lo, hi, 0, 1, 2, 3, 4, 5, 6, 7);
      }
    };
    ldw(0); ldw(1);
    SB0();
#pragma unroll
    for (int ks = 0; ks < 16; ++ks) {
      if (ks + 2 < 16) ldw(ks + 2);
#pragma unroll
      for (int ft = 0; ft < 2; ++ft) {
        acc[ft][0] = MFMA(wf[ks % 3][ft], xf[0][ks], acc[ft][0]);
        acc[ft][1] = MFMA(wf[ks % 3][ft], xf[1][ks], acc[ft][1]);
      }
      side(ks);
      SB0();
    }
  };
  auto preH = [&](f32x4 (&acc)[2][2], int slot, const u32x2* bv, u32 (&pa)[2][2][2]) {
#pragma unroll
    for (int ft = 0; ft < 2; ++ft) {
      f32x4 ecs = *(const LAS f32x4*)(L + M_CS + ((slot * 4 + 0) * 32 + ft * 16 + 4 * q) * 4);
#pragma unroll
      for (int rr = 1; rr < 4; ++rr) ecs += *(const LAS f32x4*)(L + M_CS + ((slot * 4 + rr) * 32 + ft * 16 + 4 * q) * 4);
      const float bj[4] = {lo_f(bv[ft][0]), hi_f(bv[ft][0]), lo_f(bv[ft][1]), hi_f(bv[ft][1])};
#pragma unroll
      for (int mt = 0; mt < 2; ++mt) {
        float v[4];
#pragma unroll
        for (int jj = 0; jj < 4; ++jj) v[jj] = acc[ft][mt][jj] * rH[mt] + (bj[jj] - mH[mt] * ecs[jj]);
        pa[mt][ft][0] = pack2(v[0], v[1]); pa[mt][ft][1] = pack2(v[2], v[3]);
      }
    }
  };
  auto actH = [&](u32 p00, u32 p01, u32 p10, u32 p11, int unit, int trow16) {
    const u32 dst = WS_H + (((stile * 2 + (r >> 1)) * 43 + unit) * 4 + (r & 1) * 2 + trow16) * 1024 + (c4 * 16 + i16) * 16;
    const u32 k00 = pack2(gelu_t(lo_f(p00)), gelu_t(hi_f(p00))), k01 = pack2(gelu_t(lo_f(p01)), gelu_t(hi_f(p01)));
    const u32 k10 = pack2(gelu_t(lo_f(p10)), gelu_t(hi_f(p10))), k11 = pack2(gelu_t(lo_f(p11)), gelu_t(hi_f(p11)));
    auto s0 = __builtin_amdgcn_permlane16_swap(k00, k10, false, false);
    auto s1 = __builtin_amdgcn_permlane16_swap(k01, k11, false, false);
    u32x4 sv = {s0[0], s1[0], s0[1], s1[1]};
    __builtin_amdgcn_raw_buffer_store_b128(sv, ws_rs, dst, 0, 16);
  };
  auto act2 = [&](u32 p) -> u32 { return pack2(gelu_t(lo_f(p)), gelu_t(hi_f(p))); };
  auto actH_fin = [&](u32 k00, u32 k01, u32 p10, u32 p11, int unit, int trow16) {
    const u32 dst = WS_H + (((stile * 2 + (r >> 1)) * 43 + unit) * 4 + (r & 1) * 2 + trow16) * 1024 + (c4 * 16 + i16) * 16;
    auto s0 = __builtin_amdgcn_permlane16_swap(k00, act2(p10), false, false);
    auto s1 = __builtin_amdgcn_permlane16_swap(k01, act2(p11), false, false);
    u32x4 sv = {s0[0], s1[0], s0[1], s1[1]};
    __builtin_amdgcn_raw_buffer_store_b128(sv, ws_rs, dst, 0, 16);
  };
  f32x4 accB[2][2], accC[2][2];
  h_mma(accB, BoolC<true>{}, 6 * BLK, 0u, g, sideB);
  WAIT_L0();
  BAR();

  const int tile = stile * 4 + nt2;
  if (g == 1) {
    __builtin_amdgcn_s_setprio(0);
    u32 pa[2][2][2];
    preH(accB, 1, bvA, pa);
    poll_flags<2>((gu64*)(ws + WS_FAB) + (((sg >> 1) * 4 + nt2) * 16 + (sg & 1) * 8) * 16 + lane, tag);
    issue_ab(0); issue_ab(1); issue_ab(2); issue_c(3);
    const u32 h_src = WS_H + (stile * 2 + (sg >> 1)) * 43 * 4096;
    const u32x4 w2_rs = mk_rs(w2, 1376 * 512 * 2);
    int wvo[4];
#pragma unroll
    for (int ii = 0; ii < 4; ++ii) {
      const int c = lane & 7, f = ((lane >> 4) & 1) << 1;
      wvo[ii] = (ii * 8 + (lane >> 3)) * 1024 + (nt2 * 128 + (sg & 1) * 64) * 2 + ((((c >> 1) ^ f) << 5) | ((c & 1) << 4)) - ii * 1024;
    }
    const int orot = stile + nt2;
    auto issue_o = [&](int j) {
      int unit;
      if (j < 4) {
        const int c = ((j + orot) & 3) * 4 + r;
        unit = c < 11 ? 3 * c + 1 : 2 * c + 12;
      } else {
        int jj = j - 4 + (orot & 3) + ((orot >> 2) & 1); jj = jj >= 6 ? jj - 6 : jj; jj = j == 10 ? 6 : jj;
        const int t = min(jj * 4 + r, 26);
        unit = t < 22 ? 3 * (t >> 1) + 2 * (t & 1) : 2 * t - 11;
      }
      DMA4S(ws_r4, lb + blkC(j) + r * 4096, lv, lv, lv, lv, h_src + unit * 4096);
      DMA4(w2_rs, lb + blkAB(j) + r * 4096, wvo[0], wvo[1], wvo[2], wvo[3], unit * 32768);
      SB0();
    };
    BAR();
    issue_ab(3);
    actH(pa[0][0][0], pa[0][0][1], pa[0][1][0], pa[0][1][1], hu0 + 1, 1);
    actH(pa[1][0][0], pa[1][0][1], pa[1][1][0], pa[1][1][1], hu0 + 1, 0);
    SB0(); WAIT_V(18); BAR();
    u32x4 pk_;
    u32 ka, kb;
    auto half_a = [&](int u, int mt) {
      pk_ = *(const LAS u32x4*)(L + 3 * BLK + ((u * 4 + r) * 2 + mt) * 1024 + lane * 16);
      ka = act2(pk_[0]); kb = act2(pk_[1]);
      asm volatile("" : "+v"(ka), "+v"(kb));
    };
    auto half_b = [&](int mt, int unit) { actH_fin(ka, kb, pk_[2], pk_[3], unit, mt); };
    SB0(); WAIT_V(14); BAR(); issue_c(4); issue_ab(4);
    half_a(0, 0);
    SB0(); WAIT_V(8); BAR();
    if (lane == 0) __hip_atomic_store((gu64*)(ws + WS_FH) + ((stile * 4 + r) * 16 + cg), tag, RLX_AGENT);
    issue_c(5); issue_ab(5);
    half_b(0, hu0);
    SB0(); WAIT_V(16); BAR(); issue_c(6); issue_ab(6);
    half_a(0, 1);
    SB0(); WAIT_V(16); BAR(); issue_c(7); issue_ab(7);
    half_b(1, hu0);
    SB0(); WAIT_V(16); BAR(); issue_c(8); issue_ab(8);
    if (nh == 3) half_a(1, 0);
    SB0(); WAIT_V(16); BAR(); issue_c(9); issue_ab(9);
    if (nh == 3) half_b(0, hu2);
    SB0(); WAIT_V(16); BAR(); issue_c(10); issue_ab(10);
    if (nh == 3) half_a(1, 1);
    SB0(); WAIT_V(16); BAR(); issue_c(11); issue_ab(11);
    if (nh == 3) half_b(1, hu2);
    SB0(); WAIT_V(16); BAR(); issue_c(12); issue_ab(12);
    {
      const int row = (stile & 7) * 172 + min(r * 64 + lane, 171);
      TOUCH(w2_rs, lb + 3 * BLK + r * 256, row * 1024 + (nt2 * 128 + (sg & 1) * 64) * 2, 0);
    }
    gu64* fhp = (gu64*)(ws + WS_FH) + (stile * 4 + 2 * (sg >> 1)) * 16 + (lane & 31);
    u64 pv;
    SB0(); WAIT_V(8); BAR();
    if (lane == 0) __hip_atomic_store((gu64*)(ws + WS_FH) + 1024 + ((stile * 4 + r) * 16 + cg), tag, RLX_AGENT);
    POLL_ISSUE(pv, fhp);
    issue_c(13); issue_ab(13);
    SB0(); WAIT_V(16); BAR(); issue_c(14); issue_ab(14);
    SB0(); WAIT_V(16); BAR(); issue_c(15); issue_ab(15);
    asm volatile("" : "+v"(pv));
    if (!__all(pv == tag)) poll_flags<1>(fhp, tag);
    SB0(); WAIT_V(16); BAR(); issue_o(0);
    SB0(); WAIT_V(16); BAR(); POLL_ISSUE(pv, fhp + 1024); issue_o(1);
    SB0(); WAIT_V(16); BAR(); issue_o(2);
    SB0(); WAIT_V(16); BAR(); issue_o(3);
    asm volatile("" : "+v"(pv));
    if (!__all(pv == tag)) poll_flags<1>(fhp + 1024, tag);
#pragma unroll 1
    for (int i = 17; i < 21; ++i) { WAIT_V(16); BAR(); issue_o(i - 13); }
    gu64* fpp = (gu64*)(ws + WS_FP) + (tile * 4 + sg) * 8 + (lane & 3);
    gu64* fall = (gu64*)(ws + WS_FAB) + ((bid == 0 && r == 0) ? lane * 8 : 0);
    u64 e0, e1, e2, e3;
    SB0(); WAIT_V(16); BAR();
    POLL_ISSUE(e0, fall); POLL_ISSUE(e1, fall + 512); POLL_ISSUE(e2, fall + 1024); POLL_ISSUE(e3, fall + 1536);
    issue_o(8);
    SB0(); WAIT_V(16); BAR(); POLL_ISSUE(pv, fpp); issue_o(9);
    SB0(); WAIT_V(16); BAR(); issue_o(10);
    SB0(); WAIT_V(16); BAR();
    asm volatile("" : "+v"(pv));
    if (!__all(pv == tag || (lane & 3) == sg)) poll_flags<1>(fpp, tag, (lane & 3) == sg);
    DMA4S(ws_r4, lb + blkC(3) + r * 4096, lv, lv, lv, lv, WS_P + (tile * 4 + sg) * 65536 + (sg ^ (2 + (r >> 1))) * 16384 + (r & 1) * 4096);
    if (r < 2) {
      DMA4S(ws_r4, lb + 3 * BLK + r * 4096, lv, lv, lv, lv, WS_P + (tile * 4 + sg) * 65536 + (sg ^ 1) * 16384 + r * 4096);
    } else {
      int xo[4];
#pragma unroll
      for (int ii = 0; ii < 4; ++ii) xo[ii] = (ii * 8 + (lane >> 3)) * 1024 + (((lane & 7) ^ ((ii * 4 + (lane >> 4)) & 7)) << 4) - ii * 1024;
      DMA4(x_rs, lb + 3 * BLK + r * 4096, xo[0], xo[1], xo[2], xo[3], ((sg >> 1) * 64 + (r - 2) * 32) * 1024 + (nt2 * 128 + (sg & 1) * 64) * 2);
    }
    SB0(); WAIT_V(16); BAR();
    SB0(); WAIT_V(8); BAR();
    SB0(); WAIT_V(0); BAR();
    if (bid == 0 && r == 0) {
      asm volatile("" : "+v"(e0), "+v"(e1), "+v"(e2), "+v"(e3));
      if (!__all(e0 == tag && e1 == tag && e2 == tag && e3 == tag)) {
        for (u32 sp = 0; sp < SPIN_CAP; ++sp) {
          bool ok = true;
#pragma unroll
          for (int k = 0; k < 4; ++k) ok &= __hip_atomic_load(fall + k * 512, RLX_AGENT) == tag;
          if (__all(ok)) break;
          __builtin_amdgcn_s_sleep(1);
        }
      }
      if (lane == 0) __hip_atomic_store((gu64*)(ws + WS_EPOCH), tag, RLX_AGENT);
    }
    BAR();
  } else {
    u32 paB[2][2][2];
    preH(accB, 0, bvA, paB);
    if (nh == 3) h_mma(accC, BoolC<false>{}, 2 * BLK, 3 * BLK, 2, [&](int) {});
    WAIT_L0();
    BAR();
#pragma unroll
    for (int mt = 0; mt < 2; ++mt) {
      u32x4 v = {paB[mt][0][0], paB[mt][0][1], paB[mt][1][0], paB[mt][1][1]};
      *(LAS u32x4*)(L + 3 * BLK + (r * 2 + mt) * 1024 + lane * 16) = v;
    }
    if (nh == 3) {
      u32 paC[2][2][2];
      preH(accC, 2, bvC, paC);
#pragma unroll
      for (int mt = 0; mt < 2; ++mt) {
        u32x4 v = {paC[mt][0][0], paC[mt][0][1], paC[mt][1][0], paC[mt][1][1]};
        *(LAS u32x4*)(L + 3 * BLK + ((4 + r) * 2 + mt) * 1024 + lane * 16) = v;
      }
    }
    const int th = r >> 1, eh = r & 1;
    const int l31 = lane & 31, hh = lane >> 5;
    f32x16 acc[2][2] = {};
    {
      const u32 a_off = eh * 4096 + ((lane >> 4) & 1) * 1024 + hh * 256 + i16 * 16;
      u32 c_off[4];
#pragma unroll
      for (int k4 = 0; k4 < 4; ++k4) c_off[k4] = (th * 64 + l31) * 128 + (((k4 * 2 + hh) ^ ((l31 >> 1) & 7)) << 4);
      bf16x8 A0[2][2], B0[2][2], A1[2][2], B1[2][2];
      auto rd = [&](bf16x8 (*A)[2], bf16x8 (*B)[2], int st, int kh) {
        LAS char* sa = L + blkAB(st) + a_off;
        LAS char* sc = L + blkC(st);
#pragma unroll
        for (int k2 = 0; k2 < 2; ++k2)
#pragma unroll
          for (int e = 0; e < 2; ++e) {
            A[k2][e] = *(const LAS bf16x8*)(sa + kh * 8192 + e * 2048 + k2 * 512);
            B[k2][e] = *(const LAS bf16x8*)(sc + c_off[kh * 2 + k2] + e * 4096);
          }
      };
      auto mm = [&](bf16x8 (*A)[2], bf16x8 (*B)[2]) {
#pragma unroll
        for (int k2 = 0; k2 < 2; ++k2)
#pragma unroll
          for (int e = 0; e < 2; ++e)
#pragma unroll
            for (int t = 0; t < 2; ++t) acc[e][t] = MFMA32(A[k2][e], B[k2][t], acc[e][t]);
      };
      auto sched = [&]() {
#pragma unroll
        for (int i = 0; i < 4; ++i) {
          __builtin_amdgcn_sched_group_barrier(0x008, 1, 0);
          __builtin_amdgcn_sched_group_barrier(0x100, 2, 0);
        }
        __builtin_amdgcn_sched_group_barrier(0x008, 4, 0);
      };
      __builtin_amdgcn_s_setprio(2);
      WAIT_V(0);
      WAIT_L0(); BAR();
      rd(A0, B0, 0, 0);
#pragma unroll 1
      for (int st = 0; st < 15; ++st) {
        rd(A1, B1, st, 1);
        mm(A0, B0);
        sched();
        WAIT_L0(); BAR();
        rd(A0, B0, st + 1, 0);
        mm(A1, B1);
        sched();
        SB0();
      }
      rd(A1, B1, 15, 1);
      mm(A0, B0);
      sched();
      WAIT_L0(); BAR();
      mm(A1, B1);
    }
    if (r == sg) {
#pragma unroll
      for (int e = 0; e < 2; ++e)
#pragma unroll
        for (int t = 0; t < 2; ++t)
#pragma unroll
          for (int rg = 0; rg < 4; ++rg) {
            f32x4 v = {acc[e][t][4 * rg], acc[e][t][4 * rg + 1], acc[e][t][4 * rg + 2], acc[e][t][4 * rg + 3]};
            *(LAS f32x4*)(L + MISC + ((e * 2 + t) * 4 + rg) * 1024 + lane * 16) = v;
          }
    }
    const u32 pdst = WS_P + ((tile * 4 + r) * 4 + sg) * 16384 + lane * 16;
    auto pstore = [&](int i) {
      if (r != sg) {
        {
          const int e = i >> 2, t = (i >> 1) & 1, h2 = i & 1;
          u32x4 v = {pack2(acc[e][t][8 * h2], acc[e][t][8 * h2 + 1]), pack2(acc[e][t][8 * h2 + 2], acc[e][t][8 * h2 + 3]),
                     pack2(acc[e][t][8 * h2 + 4], acc[e][t][8 * h2 + 5]), pack2(acc[e][t][8 * h2 + 6], acc[e][t][8 * h2 + 7])};
          __builtin_amdgcn_raw_buffer_store_b128(v, ws_rs, pdst + i * 1024, 0, 16);
        }
      }
    };
    const int dh = r & 1, t2 = r >> 1;
    const int d0 = nt2 * 128 + (sg & 1) * 64 + dh * 32 + 4 * hh;
    f32x16 oc = {};
    u32x2 bv[4];
    {
      const int a = i16 >> 2, p = lane & 3;
      const u32 wb = (8 * hh + a) * 128 + (((dh * 2 + ((lane >> 4) & 1)) ^ (a & 2)) * 32) + p * 8;
      const u32 hb = t2 * 2048 + ((lane >> 4) & 1) * 1024 + hh * 256 + i16 * 16;
      bf16x8 A0[2], B0[2], A1[2], B1[2];
      auto rd = [&](bf16x8* A, bf16x8* B, int j, int kk) {
        LAS char* sw2 = L + blkAB(j) + wb;
        LAS char* sh = L + blkC(j) + hb;
#pragma unroll
        for (int k2 = 0; k2 < 2; ++k2) {
          s16x4 lo = __builtin_amdgcn_ds_read_tr16_b64_v4i16((LAS s16x4*)(sw2 + kk * 4096 + k2 * 2048));
          s16x4 hi = __builtin_amdgcn_ds_read_tr16_b64_v4i16((LAS s16x4*)(sw2 + kk * 4096 + k2 * 2048 + 512));
          A[k2] = __builtin_shufflevector(lo, hi, 0, 1, 2, 3, 4, 5, 6, 7);
          B[k2] = *(const LAS bf16x8*)(sh + kk * 4096 + k2 * 512);
        }
      };
      auto mm = [&](bf16x8* A, bf16x8* B) {
        oc = MFMA32(A[0], B[0], oc);
        oc = MFMA32(A[1], B[1], oc);
      };
      auto sched = [&]() {
        __builtin_amdgcn_sched_group_barrier(0x100, 3, 0);
        __builtin_amdgcn_sched_group_barrier(0x008, 1, 0);
        __builtin_amdgcn_sched_group_barrier(0x100, 3, 0);
        __builtin_amdgcn_sched_group_barrier(0x008, 1, 0);
        SB0();
      };
      rd(A0, B0, 0, 0);
      SB0();
#pragma unroll
      for (int j = 0; j < 2; ++j) {
        rd(A1, B1, j, 1); mm(A0, B0); sched(); pstore(4 * j);
        rd(A0, B0, j, 2); mm(A1, B1); sched(); pstore(4 * j + 1);
        rd(A1, B1, j, 3); mm(A0, B0); sched(); pstore(4 * j + 2);
        WAIT_L0(); BAR();
        rd(A0, B0, j + 1, 0); mm(A1, B1); sched(); pstore(4 * j + 3);
      }
#pragma unroll
      for (int rg = 0; rg < 4; ++rg) {
        asm volatile("global_load_dwordx2 %0, %1, off" : "=v"(bv[rg]) : "v"(b2 + d0 + rg * 8) : "memory");
      }
#pragma unroll 1
      for (int j = 2; j < 10; ++j) {
        rd(A1, B1, j, 1); mm(A0, B0); sched();
        rd(A0, B0, j, 2); mm(A1, B1); sched();
        rd(A1, B1, j, 3); mm(A0, B0); sched();
        if (j == 3) {
          WAIT_V(0);
          if (lane == 0 && r != sg) __hip_atomic_store((gu64*)(ws + WS_FP) + (tile * 4 + r) * 8 + sg, tag, RLX_AGENT);
        }
        WAIT_L0(); BAR();
        rd(A0, B0, j + 1, 0); mm(A1, B1); sched();
      }
      rd(A1, B1, 10, 1); mm(A0, B0); sched();
      rd(A0, B0, 10, 2); mm(A1, B1); sched();
      mm(A0, B0);
    }
    WAIT_V(0);
#pragma unroll
    for (int rg = 0; rg < 4; ++rg) asm volatile("" : "+v"(bv[rg]));
    float gate[16];
#pragma unroll
    for (int rg = 0; rg < 4; ++rg) {
      const float bb[4] = {lo_f(bv[rg][0]), hi_f(bv[rg][0]), lo_f(bv[rg][1]), hi_f(bv[rg][1])};
#pragma unroll
      for (int j = 0; j < 4; ++j) {
        const float o2 = oc[4 * rg + j] + bb[j];
        gate[4 * rg + j] = o2 * sigm(o2);
      }
    }
#pragma unroll
    for (int j = 0; j < 16; j += 4) asm volatile("" : "+v"(gate[j]), "+v"(gate[j + 1]), "+v"(gate[j + 2]), "+v"(gate[j + 3]));
    BAR();
#pragma unroll
    for (int rg = 0; rg < 4; ++rg) {
      const u32 o = ((dh * 2 + t2) * 4 + rg) * 1024 + lane * 16;
      const f32x4 y0 = *(const LAS f32x4*)(L + MISC + o);
      const u32 ob = ((dh * 2 + t2) * 2 + (rg >> 1)) * 1024 + lane * 16 + (rg & 1) * 8;
      const u32x2 q1 = *(const LAS u32x2*)(L + 3 * BLK + ob), q2 = *(const LAS u32x2*)(L + 0 * BLK + ob), q3 = *(const LAS u32x2*)(L + 0 * BLK + 8192 + ob);
      LAS u32x2* xp = (LAS u32x2*)(L + 3 * BLK + 8192 + (t2 * 32 + l31) * 128 + (((dh * 4 + rg) ^ ((l31 >> 1) & 7)) << 4) + hh * 8);
      const u32x2 xv = *xp;
      const float ya[4] = {y0[0] + lo_f(q1[0]), y0[1] + hi_f(q1[0]), y0[2] + lo_f(q1[1]), y0[3] + hi_f(q1[1])};
      const float yb[4] = {lo_f(q2[0]) + lo_f(q3[0]), hi_f(q2[0]) + hi_f(q3[0]), lo_f(q2[1]) + lo_f(q3[1]), hi_f(q2[1]) + hi_f(q3[1])};
      const float xx[4] = {lo_f(xv[0]), hi_f(xv[0]), lo_f(xv[1]), hi_f(xv[1])};
      float res[4];
#pragma unroll
      for (int j = 0; j < 4; ++j) res[j] = xx[j] + (ya[j] + yb[j]) * gate[4 * rg + j];
      u32x2 ov = {pack2(res[0], res[1]), pack2(res[2], res[3])};
      *xp = ov;
    }
    WAIT_L0();
    BAR();
  }
  {
    const int row = tid >> 3, ch = tid & 7;
    const u32x4 v = *(const LAS u32x4*)(L + 3 * BLK + 8192 + row * 128 + ch * 16);
    *(u32x4*)(out + (size_t)(stile * 128 + (sg >> 1) * 64 + row) * 512 + nt2 * 128 + (sg & 1) * 64 + ((ch ^ ((row >> 1) & 7)) << 3)) = v;
  }
}

extern "C" void kernel_launch(void* const* d_in, const int* in_sizes, int n_in, void* d_out, int out_size, void* d_ws, size_t ws_size,
                              hipStream_t stream) {
  fused_k<<<256, 512, 0, stream>>>((const u16*)d_in[0], (const u16*)d_in[1], (const u16*)d_in[2], (const u16*)d_in[3], (const u16*)d_in[4],
                                   (const u16*)d_in[6], (const u16*)d_in[7], (const u16*)d_in[8], (const u16*)d_in[9], (u16*)d_out,
                                   (char*)d_ws);
}
```
